```python
import torch

def _softmax(x, axis=-1):
    return torch.softmax(x, dim=axis)
def _gelu(x):
    return torch.nn.functional.gelu(x, approximate="tanh")
def _silu(x):
    return torch.nn.functional.silu(x)
def _sigmoid(x):
    return torch.sigmoid(x)
def _layernorm(x, axis=-1, eps=1e-5):
    m = x.mean(dim=axis, keepdim=True)
    v = x.var(dim=axis, keepdim=True, unbiased=False)
    return (x - m) / torch.sqrt(v + eps)
def _scatter_add(tgt, idx, src, axis=0):
    return tgt.clone().index_add_(axis, idx, src)
def _segment_sum(src, seg, n):
    out = torch.zeros((n,) + src.shape[1:], dtype=src.dtype)
    return out.index_add_(0, seg, src)
def _pad(x, pw):
    flat = tuple(v for pair in reversed(pw) for v in pair)
    return torch.nn.functional.pad(x, flat)

kernel_name = 'sequential_preLN__sliding_block__mixer_token_mlp__prompt'

def setup_inputs():

    _w = (lambda a: (lambda t: t.to(torch.bfloat16) if t.is_floating_point() else t)(torch.as_tensor(a)))
    def _A_setup():
        B, S, D = 1, 2048, 576
        return {
            "x": _w((lambda *s: torch.randn(*s, dtype=torch.bfloat16) * 0.05)(B, S, D)),
            "wqkv": _w((lambda *s: torch.randn(*s, dtype=torch.bfloat16) * 0.05)(D, 3 * D)),
            "wo": _w((lambda *s: torch.randn(*s, dtype=torch.bfloat16) * 0.05)(D, D)),
        }
    _A_in = _A_setup()

    def _B_setup():
        B, S, D, F = 1, 2048, 576, 4608
        return {
            "x": _w((lambda *s: torch.randn(*s, dtype=torch.bfloat16) * 0.05)(B, S, D)),
            "w1": _w((lambda *s: torch.randn(*s, dtype=torch.bfloat16) * 0.8)(S, F) * (S ** -0.5)),
            "b1": _w((lambda *s: torch.randn(*s, dtype=torch.bfloat16) * 0.8)(F) * 0.02),
            "w2": _w((lambda *s: torch.randn(*s, dtype=torch.bfloat16) * 0.8)(F, S) * (F ** -0.5)),
            "b2": _w((lambda *s: torch.randn(*s, dtype=torch.bfloat16) * 0.8)(S) * 0.02),
        }
    _B_in = _B_setup()

    return {
        "A_x": _A_in["x"],
        "A_wqkv": _A_in["wqkv"],
        "A_wo": _A_in["wo"],
        "B_x": _B_in["x"],
        "B_w1": _B_in["w1"],
        "B_b1": _B_in["b1"],
        "B_w2": _B_in["w2"],
        "B_b2": _B_in["b2"],
    }

def reference(A_x, A_wqkv, A_wo, B_x, B_w1, B_b1, B_w2, B_b2):
    def _A_ref(x, wqkv, wo):
        B, S, D = x.shape
        H = 32; Dh = D // H; W = 256
        qkv = (lambda x, *d: x.permute(*d))((x @ wqkv).reshape(B, S, 3, H, Dh), 2, 0, 3, 1, 4)
        q, k, v = qkv[0], qkv[1], qkv[2]
        scores = torch.einsum("bhqd,bhkd->bhqk", q, k) * (Dh ** -0.5)
        idx = torch.arange(S)
        mask = (idx[:, None] - idx[None, :] >= 0) & (idx[:, None] - idx[None, :] < W)
        scores = torch.where(mask[None, None], scores, -1e9)
        p = _softmax(scores, axis=-1)
        o = torch.einsum("bhqk,bhkd->bhqd", p, v)
        return (lambda x, *d: x.permute(*d))(o, 0, 2, 1, 3).reshape(B, S, D) @ wo

    def _B_ref(x, w1, b1, w2, b2):
        B, S, D = x.shape
        h = _layernorm(x, axis=-1)
        u = torch.einsum("bsd,sf->bdf", h, w1) + b1
        u = _gelu(u)
        y = torch.einsum("bdf,fs->bds", u, w2) + b2
        return (lambda x, *d: x.permute(*d))(y, 0, 2, 1)

    x = A_x
    h = x + _A_ref(_layernorm(x, axis=-1), A_wqkv, A_wo)
    h2 = h + _B_ref(_layernorm(h, axis=-1), B_w1, B_b1, B_w2, B_b2)
    return h2
```

```cpp
#include <hip/hip_runtime.h>
#include <hip/hip_bf16.h>
typedef unsigned short u16;
__device__ __forceinline__ float bf2f(u16 v){ return __uint_as_float(((unsigned)v)<<16); }
__device__ __forceinline__ u16 f2bf(float f){ __hip_bfloat16 b = __float2bfloat16(f); return *(u16*)&b; }
constexpr int S=2048, D=576, F=4608, H=32, DH=18, W=256;

__global__ void ln_k(const u16* x, float* out, int twice){
  int r=blockIdx.x; float v[9]; float s=0;
  for(int i=0;i<9;i++){ v[i]=bf2f(x[r*D+threadIdx.x+64*i]); s+=v[i]; }
  for(int p=0;p<=twice;p++){
    s=0; for(int i=0;i<9;i++) s+=v[i];
    for(int o=32;o;o>>=1) s+=__shfl_xor(s,o);
    float m=s/D; float q=0; for(int i=0;i<9;i++){ v[i]-=m; q+=v[i]*v[i]; }
    for(int o=32;o;o>>=1) q+=__shfl_xor(q,o);
    float rs=rsqrtf(q/D+1e-5f); for(int i=0;i<9;i++) v[i]*=rs;
  }
  for(int i=0;i<9;i++) out[r*D+threadIdx.x+64*i]=v[i];
}
__global__ void gemm_k(const float* A, long sam, long sak, const u16* B, float* C, int M,int N,int K){
  __shared__ float As[16][64+1]; __shared__ float Bs[16][64+1];
  int tx=threadIdx.x%16, ty=threadIdx.x/16; int m0=blockIdx.y*64, n0=blockIdx.x*64;
  float acc[4][4]={};
  for(int k0=0;k0<K;k0+=16){
    for(int i=threadIdx.x;i<16*64;i+=256){ int k=i/64, m=i%64; As[k][m]=A[(m0+m)*sam+(k0+k)*sak]; Bs[k][m]=bf2f(B[(long)(k0+k)*N+n0+m]); }
    __syncthreads();
    for(int k=0;k<16;k++) for(int i=0;i<4;i++) for(int j=0;j<4;j++) acc[i][j]+=As[k][ty*4+i]*Bs[k][tx*4+j];
    __syncthreads();
  }
  for(int i=0;i<4;i++) for(int j=0;j<4;j++) C[(long)(m0+ty*4+i)*N+n0+tx*4+j]=acc[i][j];
}
__global__ void attn_k(const float* qkv, float* o){
  int q=blockIdx.x, h=blockIdx.y; int lane=threadIdx.x;
  float qv[DH]; for(int d=0;d<DH;d++) qv[d]=qkv[q*3*D+h*DH+d];
  float sc[4]; float mx=-1e30f;
  for(int i=0;i<4;i++){ int k=q-(lane+64*i); float s=-1e30f; if(k>=0){ s=0; for(int d=0;d<DH;d++) s+=qv[d]*qkv[k*3*D+D+h*DH+d]; s*=rsqrtf((float)DH);} sc[i]=s; mx=fmaxf(mx,s);}
  for(int of=32;of;of>>=1) mx=fmaxf(mx,__shfl_xor(mx,of));
  float sum=0; for(int i=0;i<4;i++){ sc[i]=__expf(sc[i]-mx); sum+=sc[i]; }
  for(int of=32;of;of>>=1) sum+=__shfl_xor(sum,of);
  for(int d=0;d<DH;d++){ float a=0; for(int i=0;i<4;i++){ int k=q-(lane+64*i); if(k>=0) a+=sc[i]*qkv[k*3*D+2*D+h*DH+d]; }
    for(int of=32;of;of>>=1) a+=__shfl_xor(a,of); if(lane==0) o[q*D+h*DH+d]=a/sum; }
}
__global__ void res_k(const u16* x, const float* a, u16* h){ int i=blockIdx.x*256+threadIdx.x; h[i]=f2bf(bf2f(x[i])+a[i]); }
__global__ void gelu_k(float* u, const u16* b1){ int i=blockIdx.x*256+threadIdx.x; float v=u[i]+bf2f(b1[i%F]); u[i]=0.5f*v*(1.f+tanhf(0.7978845608f*(v+0.044715f*v*v*v))); }
__global__ void fin_k(const u16* h, const float* y, const u16* b2, u16* out){ int i=blockIdx.x*256+threadIdx.x; int s=i/D, d=i%D; out[i]=f2bf(bf2f(h[i])+y[d*S+s]+bf2f(b2[s])); }

extern "C" void kernel_launch(void* const* d_in, const int* in_sizes, int n_in, void* d_out, int out_size, void* d_ws, size_t ws_size, hipStream_t stream) {
  const u16* x=(const u16*)d_in[0]; const u16* wqkv=(const u16*)d_in[1]; const u16* wo=(const u16*)d_in[2];
  const u16* w1=(const u16*)d_in[4]; const u16* b1=(const u16*)d_in[5]; const u16* w2=(const u16*)d_in[6]; const u16* b2=(const u16*)d_in[7];
  float* ws=(float*)d_ws; float* xn=ws; float* qkv=xn+S*D; float* o=qkv+S*3*D; float* a=o+S*D; u16* h=(u16*)(a+S*D); float* hn=a+2*S*D; float* u=hn+S*D; float* y=u+D*F;
  ln_k<<<S,64,0,stream>>>(x,xn,0);
  gemm_k<<<dim3(3*D/64,S/64),256,0,stream>>>(xn,D,1,wqkv,qkv,S,3*D,D);
  attn_k<<<dim3(S,H),64,0,stream>>>(qkv,o);
  gemm_k<<<dim3(D/64,S/64),256,0,stream>>>(o,D,1,wo,a,S,D,D);
  res_k<<<S*D/256,256,0,stream>>>(x,a,h);
  ln_k<<<S,64,0,stream>>>(h,hn,1);
  gemm_k<<<dim3(F/64,D/64),256,0,stream>>>(hn,1,D,w1,u,D,F,S);
  gelu_k<<<D*F/256,256,0,stream>>>(u,b1);
  gemm_k<<<dim3(S/64,D/64),256,0,stream>>>(u,F,1,w2,y,D,S,F);
  fin_k<<<S*D/256,256,0,stream>>>(h,y,b2,(u16*)d_out);
}
```

```cpp
#include <hip/hip_runtime.h>
#include <hip/hip_bf16.h>
typedef unsigned short u16;
__device__ __forceinline__ float bf2f(u16 v){ return __uint_as_float(((unsigned)v)<<16); }
__device__ __forceinline__ u16 f2bf(float f){ __hip_bfloat16 b = __float2bfloat16(f); return *(u16*)&b; }
constexpr int S=2048, D=576, F=4608, H=32, DH=18, W=256;

__global__ void ln_k(const u16* x, float* out, int twice){
  int r=blockIdx.x; float v[9]; float s=0;
  for(int i=0;i<9;i++){ v[i]=bf2f(x[r*D+threadIdx.x+64*i]); s+=v[i]; }
  for(int p=0;p<=twice;p++){
    s=0; for(int i=0;i<9;i++) s+=v[i];
    for(int o=32;o;o>>=1) s+=__shfl_xor(s,o);
    float m=s/D; float q=0; for(int i=0;i<9;i++){ v[i]-=m; q+=v[i]*v[i]; }
    for(int o=32;o;o>>=1) q+=__shfl_xor(q,o);
    float rs=rsqrtf(q/D+1e-5f); for(int i=0;i<9;i++) v[i]*=rs;
  }
  for(int i=0;i<9;i++) out[r*D+threadIdx.x+64*i]=v[i];
}
__global__ void gemm_k(const float* A, long sam, long sak, const u16* B, float* C, int M,int N,int K){
  __shared__ float As[16][64+1]; __shared__ float Bs[16][64+1];
  int tx=threadIdx.x%16, ty=threadIdx.x/16; int m0=blockIdx.y*64, n0=blockIdx.x*64;
  float acc[4][4]={};
  for(int k0=0;k0<K;k0+=16){
    for(int i=threadIdx.x;i<16*64;i+=256){ int k=i/64, m=i%64; As[k][m]=A[(m0+m)*sam+(k0+k)*sak]; Bs[k][m]=bf2f(B[(long)(k0+k)*N+n0+m]); }
    __syncthreads();
    for(int k=0;k<16;k++) for(int i=0;i<4;i++) for(int j=0;j<4;j++) acc[i][j]+=As[k][ty*4+i]*Bs[k][tx*4+j];
    __syncthreads();
  }
  for(int i=0;i<4;i++) for(int j=0;j<4;j++) C[(long)(m0+ty*4+i)*N+n0+tx*4+j]=acc[i][j];
}
__global__ void attn_k(const float* qkv, float* o){
  int q=blockIdx.x, h=blockIdx.y; int lane=threadIdx.x;
  float qv[DH]; for(int d=0;d<DH;d++) qv[d]=qkv[q*3*D+h*DH+d];
  float sc[4]; float mx=-1e30f;
  for(int i=0;i<4;i++){ int k=q-(lane+64*i); float s=-1e30f; if(k>=0){ s=0; for(int d=0;d<DH;d++) s+=qv[d]*qkv[k*3*D+D+h*DH+d]; s*=rsqrtf((float)DH);} sc[i]=s; mx=fmaxf(mx,s);}
  for(int of=32;of;of>>=1) mx=fmaxf(mx,__shfl_xor(mx,of));
  float sum=0; for(int i=0;i<4;i++){ sc[i]=__expf(sc[i]-mx); sum+=sc[i]; }
  for(int of=32;of;of>>=1) sum+=__shfl_xor(sum,of);
  for(int d=0;d<DH;d++){ float a=0; for(int i=0;i<4;i++){ int k=q-(lane+64*i); if(k>=0) a+=sc[i]*qkv[k*3*D+2*D+h*DH+d]; }
    for(int of=32;of;of>>=1) a+=__shfl_xor(a,of); if(lane==0) o[q*D+h*DH+d]=a/sum; }
}
__global__ void res_k(const u16* x, const float* a, u16* h){ int i=blockIdx.x*256+threadIdx.x; h[i]=f2bf(bf2f(x[i])+a[i]); }
__global__ void gelu_k(float* u, const u16* b1){ int i=blockIdx.x*256+threadIdx.x; float v=u[i]+bf2f(b1[i%F]); u[i]=0.5f*v*(1.f+tanhf(0.7978845608f*(v+0.044715f*v*v*v))); }
__global__ void fin_k(const u16* h, const float* y, const u16* b2, u16* out){ int i=blockIdx.x*256+threadIdx.x; int s=i/D, d=i%D; out[i]=f2bf(bf2f(h[i])+y[d*S+s]+bf2f(b2[s])); }


__global__ __launch_bounds__(512) void p1_k(unsigned long long* ts, float* sink){
  unsigned long long t0=__builtin_amdgcn_s_memrealtime();
  while(__builtin_amdgcn_s_memrealtime()-t0 < 300) __builtin_amdgcn_s_sleep(1);
  sink[blockIdx.x*512+threadIdx.x]=1.0f;
  __syncthreads();
  if(threadIdx.x==0){ ts[blockIdx.x]=__builtin_amdgcn_s_memrealtime(); ts[768+blockIdx.x]=t0; }
}
__global__ __launch_bounds__(512) void p2_k(unsigned long long* ts, float* sink){
  unsigned long long t0=__builtin_amdgcn_s_memrealtime();
  float v=threadIdx.x;
  asm volatile(".rept 16\n v_fma_f32 %0, %0, %0, %0\n .endr":"+v"(v));
  unsigned long long t1=__builtin_amdgcn_s_memrealtime();
  if(v==123.f) sink[0]=v;
  if(threadIdx.x==0){ ts[256+blockIdx.x]=t0; ts[512+blockIdx.x]=t1; }
}
__global__ void p3_k(unsigned long long* ts){
  unsigned long long e=0,smin=~0ull,smax=0,dmax=0,s1min=~0ull,s1max=0;
  for(int i=0;i<256;i++){ e=e>ts[i]?e:ts[i]; smin=smin<ts[256+i]?smin:ts[256+i]; smax=smax>ts[256+i]?smax:ts[256+i]; unsigned long long d=ts[512+i]-ts[256+i]; dmax=dmax>d?dmax:d;
     s1min=s1min<ts[768+i]?s1min:ts[768+i]; s1max=s1max>ts[768+i]?s1max:ts[768+i]; }
  unsigned long long d = 0==0 ? smin-e : 0==1 ? smax-e : 0==2 ? dmax : s1max-s1min;
  if(d>5000) d=5000;
  unsigned long long t1=__builtin_amdgcn_s_memrealtime();
  while(__builtin_amdgcn_s_memrealtime()-t1 < d*500) __builtin_amdgcn_s_sleep(32);
}
extern "C" void kernel_launch(void* const* d_in, const int* in_sizes, int n_in, void* d_out, int out_size, void* d_ws, size_t ws_size, hipStream_t stream) {
  const u16* x=(const u16*)d_in[0]; const u16* wqkv=(const u16*)d_in[1]; const u16* wo=(const u16*)d_in[2];
  const u16* w1=(const u16*)d_in[4]; const u16* b1=(const u16*)d_in[5]; const u16* w2=(const u16*)d_in[6]; const u16* b2=(const u16*)d_in[7];
  float* ws=(float*)d_ws; float* xn=ws; float* qkv=xn+S*D; float* o=qkv+S*3*D; float* a=o+S*D; u16* h=(u16*)(a+S*D); float* hn=a+2*S*D; float* u=hn+S*D; float* y=u+D*F;
  unsigned long long* ts=(unsigned long long*)d_ws+25000000; float* sk=(float*)d_ws+51000000;
  p1_k<<<256,512,0,stream>>>(ts,sk); p2_k<<<256,512,0,stream>>>(ts,sk); p3_k<<<1,64,0,stream>>>(ts);
  ln_k<<<S,64,0,stream>>>(x,xn,0);
  gemm_k<<<dim3(3*D/64,S/64),256,0,stream>>>(xn,D,1,wqkv,qkv,S,3*D,D);
  attn_k<<<dim3(S,H),64,0,stream>>>(qkv,o);
  gemm_k<<<dim3(D/64,S/64),256,0,stream>>>(o,D,1,wo,a,S,D,D);
  res_k<<<S*D/256,256,0,stream>>>(x,a,h);
  ln_k<<<S,64,0,stream>>>(h,hn,1);
  gemm_k<<<dim3(F/64,D/64),256,0,stream>>>(hn,1,D,w1,u,D,F,S);
  gelu_k<<<D*F/256,256,0,stream>>>(u,b1);
  gemm_k<<<dim3(S/64,D/64),256,0,stream>>>(u,F,1,w2,y,D,S,F);
  fin_k<<<S*D/256,256,0,stream>>>(h,y,b2,(u16*)d_out);
}
```

```cpp
#include <hip/hip_runtime.h>
typedef unsigned short u16;
typedef unsigned long long u64;
typedef short s16x4 __attribute__((ext_vector_type(4)));
typedef short s16x8 __attribute__((ext_vector_type(8)));
typedef float f32x2 __attribute__((ext_vector_type(2)));
typedef float f32x4 __attribute__((ext_vector_type(4)));
typedef unsigned u32x4 __attribute__((ext_vector_type(4)));
typedef unsigned u32x2 __attribute__((ext_vector_type(2)));
typedef __bf16 b16x2 __attribute__((ext_vector_type(2)));
#define LAS __attribute__((address_space(3)))
#define GAS __attribute__((address_space(1)))
#define DI __device__ __forceinline__
#define SB0 __builtin_amdgcn_sched_barrier(0)
#define UNR _Pragma("unroll")

constexpr int WS_QKV = 0;
constexpr int WS_OM  = 7077888;
constexpr int WS_OT  = WS_OM + 2097152;
constexpr int WS_HN  = WS_OT + 262144;
constexpr int WS_HF  = WS_HN + 2359296;
constexpr int WS_U   = WS_HF + 2359296;
constexpr int WS_PART= WS_U + 5308416;
constexpr int WS_ST  = WS_PART + 18874368;
constexpr int WS_FL  = WS_ST + 393216;
constexpr int FL_A=0, FL_B=256, FL_C=512, FL_D=768, FL_E=1024, FL_EPOCH=2048;
constexpr int WS_TS  = WS_FL + 4096*128;
constexpr int SPIN_MAX = 1<<20;

template<int N> DI void wait_vm(){ asm volatile("s_waitcnt vmcnt(%0)"::"n"(N):"memory"); }
DI void wait_lgkm0(){ asm volatile("s_waitcnt lgkmcnt(0)":::"memory"); }
DI void bar(){ asm volatile("":::"memory"); __builtin_amdgcn_s_barrier(); asm volatile("":::"memory"); }
typedef __amdgpu_buffer_rsrc_t RS;
DI RS mkrs(const void* p,int bytes){ return __builtin_amdgcn_make_buffer_rsrc((void*)p,0,bytes,0x00020000); }
template<int AUX> DI void glds16(RS r,int voff,int soff,LAS char* l){ __builtin_amdgcn_raw_ptr_buffer_load_lds(r,(LAS void*)l,16,voff,soff,0,AUX); }
DI s16x4 trread(LAS char* p){ return __builtin_amdgcn_ds_read_tr16_b64_v4i16((LAS s16x4*)p); }
DI s16x8 cat(s16x4 a, s16x4 b){ return __builtin_shufflevector(a,b,0,1,2,3,4,5,6,7); }
DI f32x4 mfma(s16x8 a, s16x8 b, f32x4 c){ return __builtin_amdgcn_mfma_f32_16x16x32_bf16(a,b,c,0,0,0); }
DI unsigned pk(float a, float b){ f32x2 v={a,b}; b16x2 r=__builtin_convertvector(v,b16x2); return __builtin_bit_cast(unsigned,r); }
DI float blo(unsigned u){ return __uint_as_float(u<<16); }
DI float bhi(unsigned u){ return __uint_as_float(u&0xffff0000u); }
DI void st16(RS r,int voff,int soff,u32x4 v){ __builtin_amdgcn_raw_buffer_store_b128(v,r,voff,soff,16); }
DI void st8(RS r,int voff,int soff,u32x2 v){ __builtin_amdgcn_raw_buffer_store_b64(v,r,voff,soff,16); }
DI u32x4 ld16(RS r,int voff,int soff){ return __builtin_amdgcn_raw_buffer_load_b128(r,voff,soff,16); }
DI void setflag(RS ws,int idx,u32x2 tag){ st8(ws,WS_FL+idx*128,0,tag); }
DI void waitflag(RS ws,int idx,u32x2 tag){
  for(int i=0;i<SPIN_MAX;i++){ asm volatile("":::"memory"); u32x2 v=__builtin_amdgcn_raw_buffer_load_b64(ws,WS_FL+idx*128,0,16);
    if(__all(v[0]==tag[0]&&v[1]==tag[1])) break; __builtin_amdgcn_s_sleep(1); }
  asm volatile("":::"memory");
}
template<int CTRL> DI float dppadd(float v){ return v+__int_as_float(__builtin_amdgcn_update_dpp(0,__float_as_int(v),CTRL,0xf,0xf,true)); }
DI float rowsum16(float v){ v=dppadd<0x128>(v); v=dppadd<0x124>(v); v=dppadd<0x122>(v); v=dppadd<0x121>(v); return v; }
DI float x16(float v,bool mx){ auto r=__builtin_amdgcn_permlane16_swap(__float_as_uint(v),__float_as_uint(v),false,false); float a=__uint_as_float(r[0]),b=__uint_as_float(r[1]); return mx?fmaxf(a,b):a+b; }
DI float x32(float v,bool mx){ auto r=__builtin_amdgcn_permlane32_swap(__float_as_uint(v),__float_as_uint(v),false,false); float a=__uint_as_float(r[0]),b=__uint_as_float(r[1]); return mx?fmaxf(a,b):a+b; }

template<int NBUF,int NST,int CNT,class F> DI void loader_sched(F issue){
  UNR for(int s=0;s<NBUF-1;s++) issue(s,s);
  wait_vm<(NBUF-2)*CNT>(); bar();
  int buf=NBUF-1;
  for(int s=NBUF-1;s<NST;s++){ issue(s,buf); buf=(buf+1==NBUF)?0:buf+1; wait_vm<(NBUF-2)*CNT>(); bar(); }
  if(NBUF>7){ wait_vm<5*CNT>(); bar(); } if(NBUF>6){ wait_vm<4*CNT>(); bar(); }
  if(NBUF>5){ wait_vm<3*CNT>(); bar(); } if(NBUF>4){ wait_vm<2*CNT>(); bar(); }
  if(NBUF>3){ wait_vm<CNT>(); bar(); } if(NBUF>2){ wait_vm<0>(); bar(); }
}

DI void phaseA(LAS char* sm,RS x,RS wqkv,RS ws,int wid,int wave,int lane){
  const int xcd=wid&7,i5=wid>>3, tb=(xcd>>1)*4+(i5&3), cb=(xcd&1)*8+(i5>>2);
  const int g=lane>>4,tok=lane&15;
  f32x4 acc[2][7]={}, gram[2]={}, cs[2]={};
  if(wave>=4){
    const int j=wave-4; int rot=((i5&3)*2+(i5>>2))%9;
    int xp[4],wp[4]; bool act[2];
    UNR for(int i=0;i<4;i++){ int row=8*(4*j+i)+(lane>>3); xp[i]=(tb*128+row)*1152+(((lane&7)^((row>>1)&7))*16);
      int r=4*(4*j+i)+g; int lc=tok^(((r&3)|((r>>1)&4))<<1); act[i>>1]=lc<14; wp[i]=r*3456+cb*216+lc*16-(lc==13?8:0); }
    loader_sched<4,9,8>([&](int s,int buf){
      LAS char* b=sm+buf*32768+j*4096;
      UNR for(int i=0;i<4;i++) glds16<0>(x,xp[i],rot*128,b+i*1024);
      if(act[0]){ glds16<0>(wqkv,wp[0],rot*(64*3456),b+16384); glds16<0>(wqkv,wp[1],rot*(64*3456),b+16384+1024); }
      if(act[1]){ glds16<0>(wqkv,wp[2],rot*(64*3456),b+16384+2048); glds16<0>(wqkv,wp[3],rot*(64*3456),b+16384+3072); }
      rot=(rot==8)?0:rot+1; });
  } else {
    { int r=lane; *(LAS u32x4*)(sm+wave*32768+16384+r*256+((14^(((r&3)|((r>>1)&4))<<1))*16))=(u32x4){0x3f803f80u,0x3f803f80u,0x3f803f80u,0x3f803f80u}; }
    const int q=tok>>2,p=lane&3,row=8*g+q,sw=((row&3)|((row>>1)&4))<<1;
    int wo_[7];
    UNR for(int j=0;j<7;j++){ int nt=j+2*wave; nt=nt>=7?nt-7:nt; int lc=2*nt+(p>>1),sub=(p&1)*8; if(nt==6&&p>=2){ lc=11+p; sub=p==2?8:0; }
      wo_[j]=16384+row*256+(lc^sw)*16+sub; }
    const int xo0=(32*wave+tok)*128+((g^((tok>>1)&7))*16), xo1=xo0^64;
    const s16x8 ones={0x3f80,0x3f80,0x3f80,0x3f80,0x3f80,0x3f80,0x3f80,0x3f80};
    s16x8 wb[2][7],xf[2][2];
    auto rd=[&](int slot,LAS char* b,int ks){
      UNR for(int t=0;t<2;t++) xf[slot][t]=*(LAS s16x8*)(b+(ks?xo1:xo0)+t*2048);
      UNR for(int j=0;j<7;j++) wb[slot][j]=cat(trread(b+wo_[j]+ks*8192),trread(b+wo_[j]+ks*8192+1024)); };
    auto mm=[&](int slot){
      UNR for(int t=0;t<2;t++) gram[t]=mfma(xf[slot][t],xf[slot][t],gram[t]);
      UNR for(int j=0;j<7;j++){ UNR for(int t=0;t<2;t++) acc[t][j]=mfma(wb[slot][j],xf[slot][t],acc[t][j]); if(j<2) cs[j]=mfma(wb[slot][j],ones,cs[j]); } };
    auto sched=[&](){ UNR for(int i=0;i<16;i++){ __builtin_amdgcn_sched_group_barrier(0x8,1,0); __builtin_amdgcn_sched_group_barrier(0x100,1,0);} __builtin_amdgcn_sched_group_barrier(0x8,2,0); };
    wait_lgkm0(); bar(); rd(0,sm,0); int buf=0;
    for(int s=0;s<8;s++){
      SB0; rd(1,sm+buf*32768,1); mm(0); sched(); SB0;
      wait_lgkm0(); bar(); buf=(buf+1)&3;
      SB0; rd(0,sm+buf*32768,0); mm(1); sched(); SB0; }
    rd(1,sm+buf*32768,1); mm(0); mm(1);
    UNR for(int j=0;j<2;j++){ int nt=j+2*wave; nt=nt>=7?nt-7:nt; if(tok==0) *(LAS f32x4*)(sm+32768+(nt*16+4*g)*4)=cs[j]; }
  }
  wait_lgkm0(); bar();
  if(wave<4){
    float mean[2],rs[2];
    UNR for(int t=0;t<2;t++){ float s=wave==0?acc[t][6][0]:wave==1?acc[t][4][0]:wave==2?acc[t][2][0]:acc[t][0][0];
      s=__shfl(s,48+tok); int r=lane&3; float d=r==0?gram[t][0]:r==1?gram[t][1]:r==2?gram[t][2]:gram[t][3]; d=__shfl(d,(tok>>2)*16+tok);
      mean[t]=s*(1.f/576); rs[t]=__builtin_amdgcn_rsqf(fmaxf(d*(1.f/576)-mean[t]*mean[t],0.f)+1e-5f); }
    UNR for(int j=0;j<7;j++){ int nt=j+2*wave; nt=nt>=7?nt-7:nt; f32x4 c=*(LAS f32x4*)(sm+32768+(nt*16+4*g)*4);
      UNR for(int e=0;e<2;e++){ int col=nt*16+4*g+2*e, hd=(col*57)>>10; float sc=(6*cb+hd<32)?0.34004648f:1.f;
        LAS char* a=sm+65536+hd*4572+2*col+(32*wave+tok)*36;
        UNR for(int t=0;t<2;t++){ float m=rs[t]*sc; *(LAS unsigned*)(a+t*576)=pk((acc[t][j][2*e]-mean[t]*c[2*e])*m,(acc[t][j][2*e+1]-mean[t]*c[2*e+1])*m); } } }
  }
  wait_lgkm0(); bar();
  { const int tid=wave*64+lane; const int dst=WS_QKV+cb*6*73728+tb*4608;
    UNR for(int i=0;i<4;i++){ int c=tid+512*i; if(c<1728){ int hd=(c*3641)>>20; u32x4 v=*(LAS u32x4*)(sm+65536+c*16); st16(ws,hd*(73728-4608)+c*16,dst,v); } } }
}

constexpr int LB_K=110592, LB_V=LB_K+24576;
template<int AUX> DI void phaseB(LAS char* sm,RS ws,int wid,int wave,int lane){
  const int head=(wid&7)*4+((wid>>3)&3), tb=wid>>5, g=lane>>4, qq=lane&15;
  const int qb=WS_QKV+head*73728;
  UNR for(int i=0;i<3;i++){ int id=i*64+lane, r=(id*171)>>9, c=id-3*r; int t=tb*256-256+64*wave+r; t=t<0?0:t;
    const int s=t*36+(c==2?20:c*16);
    glds16<AUX>(ws,s,qb+32*73728,sm+LB_K+(wave*3+i)*1024); glds16<AUX>(ws,s,qb+64*73728,sm+LB_V+(wave*3+i)*1024); }
  u32x4 qf[2];
  UNR for(int u=0;u<2;u++) qf[u]=ld16(ws,(16*u+qq)*36+(g>=2?20:g*16),qb+(tb*256+32*wave)*36);
  wait_vm<0>();
  *(LAS u32x2*)(sm+LB_V+(wave*64+lane)*48+32)=(u32x2){0x3f803f80u,0x3f803f80u};
  wait_lgkm0(); bar();
  UNR for(int u=0;u<2;u++){ if(g>=2){ qf[u][0]=0; qf[u][1]=0; qf[u][2]=0; } if(g==3) qf[u][3]=0; }
  const unsigned mk0=(4*g>qq?0xffffu:0u)|(4*g+1>qq?0xffff0000u:0u), mk1=(4*g+2>qq?0xffffu:0u)|(4*g+3>qq?0xffff0000u:0u);
  u32x2 om={0,0}; unsigned ot=0; u32x4 qc=qf[0];
  #pragma unroll 1
  for(int u=0;u<2;u++){
    const int row0=32*wave+16*u;
    LAS char* kp=sm+LB_K+(row0+qq)*48+(g>=2?32:g*16);
    const int p=lane&3, vr=(row0+4*g+(qq>>2))*48;
    LAS char* vp=sm+LB_V+vr+p*8; LAS char* vt=sm+LB_V+vr+(p==0?40:32);
    f32x4 S[17];
    UNR for(int T=0;T<17;T++) S[T]=mfma(*(LAS s16x8*)(kp+T*768),__builtin_bit_cast(s16x8,qc),(f32x4){0,0,0,0});
    if(tb==0){ int ninv=16-2*wave-u; UNR for(int T=0;T<16;T++) if(T<ninv) S[T]=(f32x4){-3e38f,-3e38f,-3e38f,-3e38f}; }
    UNR for(int r=0;r<4;r++) S[0][r]=(4*g+r>qq)?S[0][r]:S[16][r];
    float m=fmaxf(S[0][0],S[0][1]); m=fmaxf(fmaxf(m,S[0][2]),S[0][3]);
    UNR for(int T=1;T<16;T++){ m=fmaxf(fmaxf(m,S[T][0]),S[T][1]); m=fmaxf(fmaxf(m,S[T][2]),S[T][3]); }
    m=x16(m,true); m=x32(m,true);
    u32x2 P[16];
    UNR for(int T=0;T<16;T++){ P[T][0]=pk(__builtin_amdgcn_exp2f(S[T][0]-m),__builtin_amdgcn_exp2f(S[T][1]-m)); P[T][1]=pk(__builtin_amdgcn_exp2f(S[T][2]-m),__builtin_amdgcn_exp2f(S[T][3]-m)); }
    f32x4 o={0,0,0,0},t4={0,0,0,0};
    UNR for(int k=0;k<9;k++){
      const int Ta=k==0?0:2*k-1, Tb=k==0?16:(k==8?15:2*k);
      u32x4 pb= k==0? (u32x4){P[0][0]&mk0,P[0][1]&mk1,P[0][0]&~mk0,P[0][1]&~mk1} : k==8? (u32x4){P[15][0],P[15][1],0,0} : (u32x4){P[Ta][0],P[Ta][1],P[Tb&15][0],P[Tb&15][1]};
      o=mfma(cat(trread(vp+Ta*768),trread(vp+Tb*768)),__builtin_bit_cast(s16x8,pb),o);
      t4=mfma(cat(trread(vt+Ta*768),trread(vt+Tb*768)),__builtin_bit_cast(s16x8,pb),t4);
    }
    float inv=__builtin_amdgcn_rcpf(__shfl(t4[0],16+qq));
    u32x2 c={pk(o[0]*inv,o[1]*inv),pk(o[2]*inv,o[3]*inv)}; unsigned ct=pk(t4[2]*inv,t4[3]*inv);
    if(u==1){ st16(ws,lane*16,WS_OM+head*65536+(tb*8+wave)*1024,(u32x4){om[0],om[1],c[0],c[1]});
      if(g==0) st8(ws,lane*8,WS_OT+head*8192+(tb*8+wave)*128,(u32x2){ot,ct}); }
    om=c; ot=ct; qc=qf[1];
  }
}

constexpr int LC_R=110592, LC_X=LC_R+40960;
DI void phaseC_wo(LAS char* sm,RS wo,int wid,int wave,int lane){
  const int db=wid>>5;
  for(int pc=wave;pc<108;pc+=8){ int id=pc*64+lane, r=(id*5462)>>16, c=id-12*r; c^=((r>>2)&1)<<1;
    int src; if(r<512) src=(r>>4)*18+(r&15); else { int rr=r-512, t=rr>>5, hi=(rr>>4)&1, gq=rr&15; src=(16*t+(gq&12)+2*hi+((gq>>1)&1))*18+16+(gq&1); }
    glds16<0>(wo,src*1152+c*16,db*192,sm+pc*1024); }
}
DI void phaseC_x(LAS char* sm,RS x,int wid,int wave,int lane){
  const int db=wid>>5, tb=(wid&7)*4+((wid>>3)&3);
  for(int pc=wave;pc<12;pc+=8){ int id=pc*64+lane, r=(id*5462)>>16, c=id-12*r; c^=((r>>2)&1)<<1;
    glds16<0>(x,r*1152+c*16,tb*64*1152+db*192,sm+LC_X+pc*1024); }
}
template<int AUX> DI void phaseC(LAS char* sm,RS ws,int wid,int wave,int lane,unsigned tag){
  const int db=wid>>5, tb=(wid&7)*4+((wid>>3)&3), g=lane>>4, tok=lane&15;
  if(wave>=4){
    const int j=wave-4;
    const int mp=WS_OM+j*65536+tb*2048, tp=WS_OT+8*j*8192+tb*256;
    loader_sched<5,9,2>([&](int s,int buf){ LAS char* b=sm+LC_R+buf*8192+j*2048;
      const int v=s<8?lane*16:g*8192+tok*16, a=s<8?mp+s*262144:tp; glds16<AUX>(ws,v,a,b); glds16<AUX>(ws,v,a+(s<8?1024:32768),b+1024); });
  } else {
    const int pr=wave&1, dh=wave>>1, q=tok>>2, p=lane&3;
    int wof[3]; UNR for(int j=0;j<3;j++) wof[j]=(4*g+q)*192+((((3*dh+j)*2+(p>>1))^((g&1)<<1))*16)+(p&1)*8;
    f32x4 acc[2][3];
    wait_vm<0>(); bar();
    UNR for(int t=0;t<2;t++) UNR for(int j=0;j<3;j++){ u32x2 v=__builtin_bit_cast(u32x2,trread(sm+LC_X+(32*pr+16*t)*192+wof[j])); acc[t][j]=(f32x4){blo(v[0]),bhi(v[0]),blo(v[1]),bhi(v[1])}; }
    int buf=0; LAS char* wbase=sm;
    #pragma unroll 1
    for(int s=0;s<8;s++){
      LAS char* b=sm+LC_R+buf*8192+pr*1024+lane*16;
      UNR for(int ks=0;ks<2;ks++){ s16x8 a[2],w[3];
        UNR for(int t=0;t<2;t++) a[t]=cat(*(LAS s16x4*)(b+ks*4096+t*8),*(LAS s16x4*)(b+ks*4096+2048+t*8));
        UNR for(int j=0;j<3;j++) w[j]=cat(trread(wbase+ks*6144+wof[j]),trread(wbase+ks*6144+3072+wof[j]));
        UNR for(int j=0;j<3;j++) UNR for(int t=0;t<2;t++) acc[t][j]=mfma(a[t],w[j],acc[t][j]); }
      wbase+=12288; buf=buf==4?0:buf+1; wait_lgkm0(); bar();
    }
    { LAS char* b=sm+LC_R+buf*8192+g*1024+pr*128+tok*8;
      UNR for(int ks=0;ks<2;ks++){ s16x8 w[3]; u32x2 v[4];
        UNR for(int k=0;k<4;k++) v[k]=*(LAS u32x2*)(b+ks*4096+k*256);
        UNR for(int j=0;j<3;j++) w[j]=cat(trread(wbase+ks*6144+wof[j]),trread(wbase+ks*6144+3072+wof[j]));
        UNR for(int j=0;j<3;j++) UNR for(int t=0;t<2;t++) acc[t][j]=mfma(__builtin_bit_cast(s16x8,((u32x4){v[0][t],v[1][t],v[2][t],v[3][t]})),w[j],acc[t][j]); } }
    u32x4 hb[3];
    UNR for(int j=0;j<3;j++){ UNR for(int t=0;t<2;t++){ hb[j][2*t]=pk(acc[t][j][0],acc[t][j][1]); hb[j][2*t+1]=pk(acc[t][j][2],acc[t][j][3]);
        acc[t][j]=(f32x4){blo(hb[j][2*t]),bhi(hb[j][2*t]),blo(hb[j][2*t+1]),bhi(hb[j][2*t+1])}; } }
    float sm_=0,sq_=0;
    UNR for(int t=0;t<2;t++) UNR for(int r=0;r<4;r++){ float mu=rowsum16(acc[t][0][r]+acc[t][1][r]+acc[t][2][r])*(1.f/48);
      float a=acc[t][0][r]-mu,b=acc[t][1][r]-mu,c=acc[t][2][r]-mu; float m2=rowsum16(a*a+b*b+c*c); if(tok==4*t+r){ sm_=mu; sq_=m2; } }
    const int sb=WS_ST+tb*12288+pr*512;
    if(tok<8) st16(ws,(16*(tok>>2)+4*g+(tok&3))*16,sb+(db*2+dh)*1024,(u32x4){__float_as_uint(sm_),tag,__float_as_uint(sq_),~tag});
    const int fo=((2*tb+pr)*36+6*db+3*dh)*1024;
    UNR for(int j=0;j<3;j++) st16(ws,lane*16+j*1024,WS_HF+fo,hb[j]);
    u32x4 gr[6]; const int gp=(lane>>5)*6144+(lane&31)*16;
    for(int it=0;it<SPIN_MAX;it++){ asm volatile("":::"memory"); UNR for(int k=0;k<6;k++) gr[k]=ld16(ws,gp+(k&3)*1024,sb+(k>>2)*4096);
      bool ok=true; UNR for(int k=0;k<6;k++) ok&=(gr[k][1]==tag)&(gr[k][3]==~tag);
      if(__all(ok)) break; }
    float mu=0; UNR for(int k=0;k<6;k++) mu+=__uint_as_float(gr[k][0]);
    mu=x32(mu,false)*(1.f/12); float m2=0;
    UNR for(int k=0;k<6;k++){ float d=__uint_as_float(gr[k][0])-mu; m2+=__uint_as_float(gr[k][2])+48.f*d*d; }
    m2=x32(m2,false)*(1.f/576);
    float r1=__builtin_amdgcn_rsqf(m2+1e-5f), rr=r1*__builtin_amdgcn_rsqf(m2*r1*r1+1e-5f);
    LAS char* sc=sm+LC_X+wave*256;
    *(LAS float*)(sc+lane*4)=lane<32?mu:rr; wait_lgkm0();
    UNR for(int t=0;t<2;t++){ f32x4 m4=*(LAS f32x4*)(sc+(16*t+4*g)*4), r4=*(LAS f32x4*)(sc+128+(16*t+4*g)*4);
      UNR for(int j=0;j<3;j++){ f32x4 z=(acc[t][j]-m4)*r4; hb[j][2*t]=pk(z[0],z[1]); hb[j][2*t+1]=pk(z[2],z[3]); } }
    UNR for(int j=0;j<3;j++) st16(ws,lane*16+j*1024,WS_HN+fo,hb[j]);
  }
}

template<int DT,int NBUF,int NST,int FC,int FAUX> DI void g_loader(LAS char* sm,RS W,int wo,int ldw,RS ws,int fo,int j,int lane){
  constexpr int SB=(8+DT)*1024;
  int wp[2]; UNR for(int i=0;i<2;i++){ int row=(j*2+i)*4+(lane>>4); wp[i]=row*ldw+((lane&15)^((row&7)<<1))*16; }
  fo+=j*1024;
  loader_sched<NBUF,NST,2+FC>([&](int s,int buf){ LAS char* b=sm+buf*SB;
    UNR for(int i=0;i<2;i++) glds16<0>(W,wp[i],wo,b+(j*2+i)*1024);
    UNR for(int i=0;i<FC;i++) glds16<FAUX>(ws,lane*16,fo+i*4096,b+8192+(j+4*i)*1024);
    wo+=32*ldw; fo+=36*1024; });
}
template<int DT,int NBUF,int NST,bool W_IS_A> DI void g_consumer(LAS char* sm,int wave,int lane,f32x4 (&acc)[DT][2]){
  constexpr int SB=(8+DT)*1024;
  const int g=lane>>4,q=(lane&15)>>2,p=lane&3,row=4*g+q;
  int wofs[2]; UNR for(int n=0;n<2;n++) wofs[n]=row*256+((((2*wave+n)*2+(p>>1))^((row&7)<<1))*16)+(p&1)*8;
  const int fofs=8192+lane*16;
  s16x8 fa[2][DT],wb[2][2];
  auto rd=[&](int slot,LAS char* b){
    UNR for(int n=0;n<2;n++) wb[slot][n]=cat(trread(b+wofs[n]),trread(b+wofs[n]+4096));
    UNR for(int d=0;d<DT;d++) fa[slot][d]=*(LAS s16x8*)(b+fofs+d*1024); };
  auto mm=[&](int slot){ UNR for(int d=0;d<DT;d++) UNR for(int n=0;n<2;n++) acc[d][n]=W_IS_A?mfma(wb[slot][n],fa[slot][d],acc[d][n]):mfma(fa[slot][d],wb[slot][n],acc[d][n]); };
  auto sched=[&](){ UNR for(int i=0;i<DT+4;i++){ __builtin_amdgcn_sched_group_barrier(0x8,1,0); __builtin_amdgcn_sched_group_barrier(0x100,1,0);} __builtin_amdgcn_sched_group_barrier(0x8,2*DT,0); };
  bar(); rd(0,sm); int buf=0;
  for(int s=0;s<NST-2;s+=2){
    wait_lgkm0(); bar(); buf=(buf+1==NBUF)?0:buf+1; SB0; rd(1,sm+buf*SB); mm(0); sched(); SB0;
    wait_lgkm0(); bar(); buf=(buf+1==NBUF)?0:buf+1; SB0; rd(0,sm+buf*SB); mm(1); sched(); SB0; }
  wait_lgkm0(); bar(); buf=(buf+1==NBUF)?0:buf+1; rd(1,sm+buf*SB); mm(0); mm(1);
}
template<int AUX> DI void phaseD(LAS char* sm,RS w1,const char* b1,RS ws,int id,int wave,int lane){
  const int db=id%6, nb=id/6;
  if(wave>=4){ const int W=nb*256, F=WS_HN+db*6144;
    if(wave<6) g_loader<6,6,64,2,AUX>(sm,w1,W,9216,ws,F,wave-4,lane); else g_loader<6,6,64,1,AUX>(sm,w1,W,9216,ws,F,wave-4,lane);
  } else {
    f32x4 acc[6][2];
    UNR for(int n=0;n<2;n++){ u32x2 v=*(const u32x2*)(b1+(nb*128+wave*32+n*16+(lane>>4)*4)*2); f32x4 b={blo(v[0]),bhi(v[0]),blo(v[1]),bhi(v[1])}; UNR for(int d=0;d<6;d++) acc[d][n]=b; }
    g_consumer<6,6,64,true>(sm,wave,lane,acc);
    const int dst=WS_U+((nb*4+wave)*36+db*6)*1024;
    UNR for(int d=0;d<6;d++){ u32x4 o;
      UNR for(int n=0;n<2;n++){ float y[4];
        UNR for(int r=0;r<4;r++){ float v=acc[d][n][r]; float e=__builtin_amdgcn_exp2f(v*(-2.3022082f-0.10294324f*v*v)); y[r]=v*__builtin_amdgcn_rcpf(1.f+e); }
        o[2*n]=pk(y[0],y[1]); o[2*n+1]=pk(y[2],y[3]); }
      st16(ws,lane*16+(d&3)*1024,dst+(d>>2)*4096,o); }
  }
}
constexpr int LE_H=6*17408, LE_ACC=LE_H+9216;
template<int AUX> DI void phaseE(LAS char* sm,RS w2,const char* b2,char* out,RS ws,int wid,int wave,int lane,u32x2 tag){
  const int xcd=wid&7,i5=wid>>3, kq=xcd>>1, sb=(xcd&1)*8+(i5>>2), db=i5&3, tile=sb*4+db, g=lane>>4;
  if(wave>=4){ const int W=kq*1152*4096+sb*256, F=WS_U+(kq*36*36+db*9)*1024;
    if(wave==4) g_loader<9,6,36,3,AUX>(sm,w2,W,4096,ws,F,0,lane); else { g_loader<9,6,36,2,AUX>(sm,w2,W,4096,ws,F,wave-4,lane);
      UNR for(int i=0;i<3;i++){ int d=(wave-5)*3+i; glds16<AUX>(ws,lane*16,WS_HF+((sb*4+kq)*36+db*9+d)*1024,sm+LE_H+d*1024); } wait_vm<0>(); }
  } else {
    f32x4 acc[9][2]={};
    g_consumer<9,6,36,false>(sm,wave,lane,acc);
    if(wave==kq){ UNR for(int d=0;d<9;d++) UNR for(int n=0;n<2;n++) *(LAS f32x4*)(sm+LE_ACC+(d*2+n)*1024+lane*16)=acc[d][n]; wait_lgkm0(); }
    else { const int dst=WS_PART+((tile*4+wave)*4+kq)*18432;
      UNR for(int d=0;d<9;d++) UNR for(int n=0;n<2;n++) st16(ws,lane*16+((d*2+n)&3)*1024,dst+((d*2+n)>>2)*4096,__builtin_bit_cast(u32x4,acc[d][n]));
      wait_vm<0>(); if(lane==0) setflag(ws,FL_E+(tile*4+wave)*4+kq,tag); }
  }
  bar();
  if(wave<6){
    { int s=lane&3; s=s==kq?(s+1)&3:s; waitflag(ws,FL_E+(tile*4+kq)*4+s,tag); }
    u32x4 pv[3][3];
    UNR for(int k=0;k<3;k++){ const int src=WS_PART+((tile*4+kq)*4+((kq+1+k)&3))*18432+wave*3072; UNR for(int i=0;i<3;i++) pv[k][i]=ld16(ws,lane*16+i*1024,src); }
    f32x4 v[3];
    UNR for(int i=0;i<3;i++){ int tl=wave*3+i,d=tl>>1,n=tl&1; u32x2 hv=__builtin_bit_cast(u32x2,trread(sm+LE_H+d*1024+((lane&3)*16+4*g+((lane&15)>>2))*16+n*8));
      float bb=blo(*(const u16*)(b2+(sb*128+kq*32+n*16+(lane&15))*2));
      v[i]=*(LAS f32x4*)(sm+LE_ACC+tl*1024+lane*16)+(f32x4){blo(hv[0]),bhi(hv[0]),blo(hv[1]),bhi(hv[1])}+bb; }
    UNR for(int i=0;i<3;i++){ int tl=wave*3+i,d=tl>>1,n=tl&1; UNR for(int k=0;k<3;k++) v[i]+=__builtin_bit_cast(f32x4,pv[k][i]);
      *(u32x2*)(out+(size_t)(sb*128+kq*32+n*16+(lane&15))*1152+(db*144+d*16+4*g)*2)=(u32x2){pk(v[i][0],v[i][1]),pk(v[i][2],v[i][3])}; }
  }
}

#define TS(k)
template<int PH> __global__ __launch_bounds__(512,2) void kern(const char* x,const char* wqkv,const char* wo,const char* w1,const char* b1,const char* w2,const char* b2,char* out,char* ws){
  extern __shared__ __attribute__((aligned(16))) char smem_[];
  LAS char* sm=(LAS char*)smem_;
  const int wave=__builtin_amdgcn_readfirstlane(threadIdx.x>>6), lane=threadIdx.x&63, wid=blockIdx.x;
  u64* ep=(u64*)(ws+WS_FL+FL_EPOCH*128);
  const RS rws=mkrs(ws,WS_TS), rx=mkrs(x,2359296);
  TS(2*PH);
  if(PH==0){ if(wid==0&&threadIdx.x==0) *ep=*ep+0x9E3779B97F4A7C15ull; phaseA(sm,rx,mkrs(wqkv,1990656),rws,wid,wave,lane); }
  if(PH==1) phaseB<0>(sm,rws,wid,wave,lane);
  if(PH==2){ u64 tag=*ep; phaseC_wo(sm,mkrs(wo,663552),wid,wave,lane); phaseC_x(sm,rx,wid,wave,lane); phaseC<0>(sm,rws,wid,wave,lane,(unsigned)tag); }
  if(PH==3) phaseD<0>(sm,mkrs(w1,18874368),b1,rws,(wid&7)*27+(wid>>3),wave,lane);
  if(PH==4){ u64 tag=*ep; phaseE<0>(sm,mkrs(w2,18874368),b2,out,rws,wid,wave,lane,__builtin_bit_cast(u32x2,tag)); }
  TS(2*PH+1);
}

extern "C" void kernel_launch(void* const* d_in,const int* in_sizes,int n_in,void* d_out,int out_size,void* d_ws,size_t ws_size,hipStream_t stream){
  const char *x=(const char*)d_in[0],*wqkv=(const char*)d_in[1],*wo=(const char*)d_in[2],*w1=(const char*)d_in[4],*b1=(const char*)d_in[5],*w2=(const char*)d_in[6],*b2=(const char*)d_in[7];
  char* out=(char*)d_out; char* ws=(char*)d_ws;
#define L(PH,N) (void)hipFuncSetAttribute((const void*)kern<PH>,hipFuncAttributeMaxDynamicSharedMemorySize,163840); kern<PH><<<N,512,163840,stream>>>(x,wqkv,wo,w1,b1,w2,b2,out,ws);
  L(0,256) L(1,256) L(2,192) L(3,216) L(4,256)
}
```

```cpp
#include <hip/hip_runtime.h>
#pragma clang diagnostic ignored "-Wint-to-pointer-cast"
typedef unsigned short u16;
typedef unsigned long long u64;
typedef short s16x4 __attribute__((ext_vector_type(4)));
typedef short s16x8 __attribute__((ext_vector_type(8)));
typedef float f32x2 __attribute__((ext_vector_type(2)));
typedef float f32x4 __attribute__((ext_vector_type(4)));
typedef unsigned u32x4 __attribute__((ext_vector_type(4)));
typedef unsigned u32x2 __attribute__((ext_vector_type(2)));
typedef __bf16 b16x2 __attribute__((ext_vector_type(2)));
#define LAS __attribute__((address_space(3)))
#define DI __device__ __forceinline__
#define SB0 __builtin_amdgcn_sched_barrier(0)
#define SGB(m,n) __builtin_amdgcn_sched_group_barrier(m,n,0)
#define UNR _Pragma("unroll")
#define LD(T,a) (*(LAS T*)(unsigned)(a))
#define LDV(T,a) (*(volatile LAS T*)(unsigned)(a))

#define EXP 0
constexpr int WS_QKV = 0;
constexpr int WS_OM  = 7077888;
constexpr int WS_OT  = WS_OM + 2097152;
constexpr int WS_HN  = WS_OT + 262144;
constexpr int WS_HF  = WS_HN + 2359296;
constexpr int WS_U   = WS_HF + 2359296;
constexpr int WS_PART= WS_U + 5308416;
constexpr int WS_ST  = WS_PART + 7077888;
constexpr int WS_RS  = WS_ST + 393216;
constexpr int WS_CS  = WS_RS + 16384;
constexpr int WS_FL  = WS_CS + 8192;
constexpr int FL_A=0, FL_B=256, FL_C=512, FL_D=768, FL_E=1024, FL_EPOCH=2048, FL_H=FL_A+192;
constexpr int WS_OV  = 33554432;
constexpr int WS_TS  = 2*WS_OV;
constexpr int SPIN_MAX = 1<<18;
constexpr int G_LF = 12*8192;

template<int N> DI void wait_vm(){ asm volatile("s_waitcnt vmcnt(%0)"::"n"(N):"memory"); }
DI void wait_lgkm0(){ asm volatile("s_waitcnt lgkmcnt(0)":::"memory"); }
DI void bar(){ asm volatile("":::"memory"); __builtin_amdgcn_s_barrier(); asm volatile("":::"memory"); }
typedef __amdgpu_buffer_rsrc_t RS;
DI RS mkrs(const void* p,int bytes){ return __builtin_amdgcn_make_buffer_rsrc((void*)p,0,bytes,0x00020000); }
DI u32x4 mkd(const void* p,int bytes){ u64 a=(u64)p; return (u32x4){(unsigned)a,(unsigned)(a>>32)&0xffffu,(unsigned)bytes,0x00020000u}; }
template<int SC1> DI void glds(u32x4 d,int voff,int soff,int lds){
  if(SC1) asm volatile("s_mov_b32 m0, %0\n\ts_nop 0\n\tbuffer_load_dwordx4 %1, %2, %3 offen sc1 lds"::"s"(lds),"v"(voff),"s"(d),"s"(soff):"memory");
  else    asm volatile("s_mov_b32 m0, %0\n\ts_nop 0\n\tbuffer_load_dwordx4 %1, %2, %3 offen lds"::"s"(lds),"v"(voff),"s"(d),"s"(soff):"memory");
}
DI void gldsc(int skip,u32x4 d,int voff,int soff,int lds){
  asm volatile("s_cmp_lg_u32 %4, 0\n\ts_cbranch_scc1 1f\n\ts_mov_b32 m0, %0\n\ts_nop 0\n\tbuffer_load_dwordx4 %1, %2, %3 offen lds\n1:"::"s"(lds),"v"(voff),"s"(d),"s"(soff),"s"(skip):"memory","scc"); }
DI s16x4 trread(int a){ return __builtin_amdgcn_ds_read_tr16_b64_v4i16((LAS s16x4*)(unsigned)a); }
DI s16x8 cat(s16x4 a, s16x4 b){ return __builtin_shufflevector(a,b,0,1,2,3,4,5,6,7); }
DI s16x8 as8(u32x4 v){ return __builtin_bit_cast(s16x8,v); }
DI f32x4 mfma(s16x8 a, s16x8 b, f32x4 c){ return __builtin_amdgcn_mfma_f32_16x16x32_bf16(a,b,c,0,0,0); }
DI unsigned pk2(f32x2 v){ return __builtin_bit_cast(unsigned,__builtin_convertvector(v,b16x2)); }
DI unsigned pk(float a, float b){ return pk2((f32x2){a,b}); }
DI float blo(unsigned u){ return __uint_as_float(u<<16); }
DI float bhi(unsigned u){ return __uint_as_float(u&0xffff0000u); }
DI f32x4 up4(unsigned a,unsigned b){ return (f32x4){blo(a),bhi(a),blo(b),bhi(b)}; }
DI void st16(RS r,int voff,int soff,u32x4 v){ __builtin_amdgcn_raw_buffer_store_b128(v,r,voff,soff,16); asm volatile("s_nop 1"::"v"(v)); }
DI void st8(RS r,int voff,int soff,u32x2 v){ __builtin_amdgcn_raw_buffer_store_b64(v,r,voff,soff,16); }
DI u32x4 ld16(RS r,int voff,int soff){ return __builtin_amdgcn_raw_buffer_load_b128(r,voff,soff,16); }
DI void setflag(RS ws,int idx,u32x2 tag){ st8(ws,(threadIdx.x&63)?0x7ffffff0:0,WS_FL+idx*128,tag); }
DI void waitflag(RS ws,int idx,u32x2 tag){
  _Pragma("unroll 1") for(int i=0;i<SPIN_MAX;i++){ asm volatile("":::"memory"); u32x2 v=__builtin_amdgcn_raw_buffer_load_b64(ws,idx*128,WS_FL,16);
    if(__all(v[0]==tag[0]&&v[1]==tag[1])) break; __builtin_amdgcn_s_sleep(1); }
  asm volatile("":::"memory");
}
template<int CTRL> DI float dpp(float v){ return __int_as_float(__builtin_amdgcn_update_dpp(0,__float_as_int(v),CTRL,0xf,0xf,true)); }
template<int CTRL> DI float dppadd(float v){ return v+__int_as_float(__builtin_amdgcn_update_dpp(0,__float_as_int(v),CTRL,0xf,0xf,true)); }
template<int N> DI float bc(float v){ return __int_as_float(__builtin_amdgcn_update_dpp(0,__float_as_int(v),0x150+N,0xf,0xf,true)); }
template<int N> DI f32x4 bc4(float v){ return (f32x4){bc<N>(v),bc<N+1>(v),bc<N+2>(v),bc<N+3>(v)}; }
DI float rowsum16(float v){ v=dppadd<0x128>(v); v=dppadd<0x124>(v); v=dppadd<0x122>(v); v=dppadd<0x121>(v); return v; }
DI float max3(float a,float b,float c){ return fmaxf(fmaxf(a,b),c); }
DI float x16(float v,bool mx){ auto r=__builtin_amdgcn_permlane16_swap(__float_as_uint(v),__float_as_uint(v),false,false); float a=__uint_as_float(r[0]),b=__uint_as_float(r[1]); return mx?fmaxf(a,b):a+b; }
DI float x32(float v,bool mx){ auto r=__builtin_amdgcn_permlane32_swap(__float_as_uint(v),__float_as_uint(v),false,false); float a=__uint_as_float(r[0]),b=__uint_as_float(r[1]); return mx?fmaxf(a,b):a+b; }

#define NA 2
#define PST 128
#define PFC 1
#define PFT 0
#define LPO 9200
#define ESL 4
#define OAUX 0
#define WOE 0
#define CV 0
#define PDL 0
#define CUP 8
#define DN1 12
#define DSL 0
#define EN1 12
#define ESL2 0
#define PFS 1
#define OPT 1
#define PSL 1
#define SDL 8
#define NBP 0
#define EPR 1
#define NC 2
#define FD1 6
#define FD2 10
#define FD3 12
#define FD4 1
#define DROT 0x66009630
#define CSL 1
#define NB 4
DI void pf1(u32x4 d,int a){ asm volatile("buffer_load_dword v255, %0, %1, 0 offen"::"v"(a),"s"(d):"v255"); }
DI void pfw(u32x4 w1,u32x4 w2,int wid,int n,int lane){
  if(n<288){ pf1(w2,((n*8+(wid&7))*64+lane)*PST); }
  else { int id=(n-288)*64+lane, k=(id*52429)>>19, c=id-10*k, t=(wid&7)*27/6+(c>>1); pf1(w1,k*9216+((t>>2)*8+(t&3)+4*(c&1))*128); } }
template<int NBUF,int NST,int CNT,class F> DI void loader_sched(F issue){
  int buf=0;
  #pragma unroll 1
  for(int s=0;s<NST;s++){ issue(s,buf); buf=(buf+1==NBUF)?0:buf+1; if(s>=NBUF-2){ wait_vm<(NBUF-2)*CNT>(); bar(); } }
  if(NBUF>4){ wait_vm<2*CNT>(); bar(); } if(NBUF>3){ wait_vm<CNT>(); bar(); } wait_vm<0>(); bar();
}

#define GA_ASM \
  "ds_read_b128 v[40:43], %[x0] offset:0\n\t" \
  "ds_read_b128 v[44:47], %[x0] offset:2048\n\t" \
  "ds_read_b64_tr_b16 v[56:57], %[w0] offset:0\n\t" \
  "ds_read_b64_tr_b16 v[58:59], %[w0] offset:2304\n\t" \
  "ds_read_b64_tr_b16 v[60:61], %[w0] offset:32\n\t" \
  "ds_read_b64_tr_b16 v[62:63], %[w0] offset:2336\n\t" \
  "ds_read_b64_tr_b16 v[64:65], %[w0] offset:64\n\t" \
  "ds_read_b64_tr_b16 v[66:67], %[w0] offset:2368\n\t" \
  "ds_read_b64_tr_b16 v[68:69], %[w0] offset:96\n\t" \
  "ds_read_b64_tr_b16 v[70:71], %[w0] offset:2400\n\t" \
  "ds_read_b64_tr_b16 v[72:73], %[w0] offset:128\n\t" \
  "ds_read_b64_tr_b16 v[74:75], %[w0] offset:2432\n\t" \
  "ds_read_b64_tr_b16 v[76:77], %[w0] offset:160\n\t" \
  "ds_read_b64_tr_b16 v[78:79], %[w0] offset:2464\n\t" \
  "ds_read_b64_tr_b16 v[80:81], %[w0] offset:192\n\t" \
  "ds_read_b64_tr_b16 v[82:83], %[w0] offset:2496\n\t" \
  "ds_read_b64_tr_b16 v[84:85], %[w0] offset:224\n\t" \
  "ds_read_b64_tr_b16 v[86:87], %[w0] offset:2528\n\t" \
  "ds_read_b64_tr_b16 v[88:89], %[w0] offset:256\n\t" \
  "ds_read_b64_tr_b16 v[90:91], %[w0] offset:2560\n\t" \
  "1:\n\t" \
  "s_waitcnt lgkmcnt(15)\n\t" \
  "v_mfma_f32_16x16x32_bf16 v[128:131], v[56:59], v[40:43], v[128:131]\n\t" \
  "ds_read_b128 v[48:51], %[x1] offset:0\n\t" \
  "v_mfma_f32_16x16x32_bf16 v[132:135], v[56:59], v[44:47], v[132:135]\n\t" \
  "ds_read_b128 v[52:55], %[x1] offset:2048\n\t" \
  "s_waitcnt lgkmcnt(15)\n\t" \
  "v_mfma_f32_16x16x32_bf16 v[136:139], v[60:63], v[40:43], v[136:139]\n\t" \
  "ds_read_b64_tr_b16 v[92:93], %[w1] offset:0\n\t" \
  "ds_read_b64_tr_b16 v[94:95], %[w1] offset:2304\n\t" \
  "v_mfma_f32_16x16x32_bf16 v[140:143], v[60:63], v[44:47], v[140:143]\n\t" \
  "ds_read_b64_tr_b16 v[96:97], %[w1] offset:32\n\t" \
  "s_add_u32 %[sb], %[sb], 34816\n\t" \
  "s_waitcnt lgkmcnt(15)\n\t" \
  "v_mfma_f32_16x16x32_bf16 v[144:147], v[64:67], v[40:43], v[144:147]\n\t" \
  "ds_read_b64_tr_b16 v[98:99], %[w1] offset:2336\n\t" \
  "s_cmp_eq_u32 %[sb], 139264\n\t" \
  "v_mfma_f32_16x16x32_bf16 v[148:151], v[64:67], v[44:47], v[148:151]\n\t" \
  "ds_read_b64_tr_b16 v[100:101], %[w1] offset:64\n\t" \
  "ds_read_b64_tr_b16 v[102:103], %[w1] offset:2368\n\t" \
  "s_waitcnt lgkmcnt(15)\n\t" \
  "v_mfma_f32_16x16x32_bf16 v[152:155], v[68:71], v[40:43], v[152:155]\n\t" \
  "ds_read_b64_tr_b16 v[104:105], %[w1] offset:96\n\t" \
  "s_cselect_b32 %[sb], 0, %[sb]\n\t" \
  "v_mfma_f32_16x16x32_bf16 v[156:159], v[68:71], v[44:47], v[156:159]\n\t" \
  "ds_read_b64_tr_b16 v[106:107], %[w1] offset:2400\n\t" \
  "v_add_u32 %[x0], %[sb], %[xb]\n\t" \
  "s_waitcnt lgkmcnt(15)\n\t" \
  "v_mfma_f32_16x16x32_bf16 v[160:163], v[72:75], v[40:43], v[160:163]\n\t" \
  "ds_read_b64_tr_b16 v[108:109], %[w1] offset:128\n\t" \
  "v_add_u32 %[w0], %[sb], %[wb]\n\t" \
  "v_mfma_f32_16x16x32_bf16 v[164:167], v[72:75], v[44:47], v[164:167]\n\t" \
  "ds_read_b64_tr_b16 v[110:111], %[w1] offset:2432\n\t" \
  "s_waitcnt lgkmcnt(15)\n\t" \
  "v_mfma_f32_16x16x32_bf16 v[168:171], v[76:79], v[40:43], v[168:171]\n\t" \
  "ds_read_b64_tr_b16 v[112:113], %[w1] offset:160\n\t" \
  "v_mfma_f32_16x16x32_bf16 v[172:175], v[76:79], v[44:47], v[172:175]\n\t" \
  "ds_read_b64_tr_b16 v[114:115], %[w1] offset:2464\n\t" \
  "s_waitcnt lgkmcnt(15)\n\t" \
  "v_mfma_f32_16x16x32_bf16 v[176:179], v[80:83], v[40:43], v[176:179]\n\t" \
  "ds_read_b64_tr_b16 v[116:117], %[w1] offset:192\n\t" \
  "v_mfma_f32_16x16x32_bf16 v[180:183], v[80:83], v[44:47], v[180:183]\n\t" \
  "ds_read_b64_tr_b16 v[118:119], %[w1] offset:2496\n\t" \
  "s_waitcnt lgkmcnt(15)\n\t" \
  "v_mfma_f32_16x16x32_bf16 v[184:187], v[84:87], v[40:43], v[184:187]\n\t" \
  "ds_read_b64_tr_b16 v[120:121], %[w1] offset:224\n\t" \
  "v_mfma_f32_16x16x32_bf16 v[188:191], v[84:87], v[44:47], v[188:191]\n\t" \
  "ds_read_b64_tr_b16 v[122:123], %[w1] offset:2528\n\t" \
  "s_waitcnt lgkmcnt(15)\n\t" \
  "v_mfma_f32_16x16x32_bf16 v[192:195], v[88:91], v[40:43], v[192:195]\n\t" \
  "ds_read_b64_tr_b16 v[124:125], %[w1] offset:256\n\t" \
  "v_mfma_f32_16x16x32_bf16 v[196:199], v[88:91], v[44:47], v[196:199]\n\t" \
  "ds_read_b64_tr_b16 v[126:127], %[w1] offset:2560\n\t" \
  "s_barrier\n\t" \
  "s_waitcnt lgkmcnt(15)\n\t" \
  "v_mfma_f32_16x16x32_bf16 v[128:131], v[92:95], v[48:51], v[128:131]\n\t" \
  "ds_read_b128 v[40:43], %[x0] offset:0\n\t" \
  "v_mfma_f32_16x16x32_bf16 v[132:135], v[92:95], v[52:55], v[132:135]\n\t" \
  "ds_read_b128 v[44:47], %[x0] offset:2048\n\t" \
  "s_waitcnt lgkmcnt(15)\n\t" \
  "v_mfma_f32_16x16x32_bf16 v[136:139], v[96:99], v[48:51], v[136:139]\n\t" \
  "ds_read_b64_tr_b16 v[56:57], %[w0] offset:0\n\t" \
  "ds_read_b64_tr_b16 v[58:59], %[w0] offset:2304\n\t" \
  "v_mfma_f32_16x16x32_bf16 v[140:143], v[96:99], v[52:55], v[140:143]\n\t" \
  "ds_read_b64_tr_b16 v[60:61], %[w0] offset:32\n\t" \
  "s_waitcnt lgkmcnt(15)\n\t" \
  "v_mfma_f32_16x16x32_bf16 v[144:147], v[100:103], v[48:51], v[144:147]\n\t" \
  "ds_read_b64_tr_b16 v[62:63], %[w0] offset:2336\n\t" \
  "v_mfma_f32_16x16x32_bf16 v[148:151], v[100:103], v[52:55], v[148:151]\n\t" \
  "ds_read_b64_tr_b16 v[64:65], %[w0] offset:64\n\t" \
  "ds_read_b64_tr_b16 v[66:67], %[w0] offset:2368\n\t" \
  "s_waitcnt lgkmcnt(15)\n\t" \
  "v_mfma_f32_16x16x32_bf16 v[152:155], v[104:107], v[48:51], v[152:155]\n\t" \
  "ds_read_b64_tr_b16 v[68:69], %[w0] offset:96\n\t" \
  "v_mfma_f32_16x16x32_bf16 v[156:159], v[104:107], v[52:55], v[156:159]\n\t" \
  "ds_read_b64_tr_b16 v[70:71], %[w0] offset:2400\n\t" \
  "v_xor_b32 %[x1], 64, %[x0]\n\t" \
  "s_waitcnt lgkmcnt(15)\n\t" \
  "v_mfma_f32_16x16x32_bf16 v[160:163], v[108:111], v[48:51], v[160:163]\n\t" \
  "ds_read_b64_tr_b16 v[72:73], %[w0] offset:128\n\t" \
  "v_add_u32 %[w1], 9216, %[w0]\n\t" \
  "v_mfma_f32_16x16x32_bf16 v[164:167], v[108:111], v[52:55], v[164:167]\n\t" \
  "ds_read_b64_tr_b16 v[74:75], %[w0] offset:2432\n\t" \
  "s_add_u32 %[it], %[it], 1\n\t" \
  "s_waitcnt lgkmcnt(15)\n\t" \
  "v_mfma_f32_16x16x32_bf16 v[168:171], v[112:115], v[48:51], v[168:171]\n\t" \
  "ds_read_b64_tr_b16 v[76:77], %[w0] offset:160\n\t" \
  "v_mfma_f32_16x16x32_bf16 v[172:175], v[112:115], v[52:55], v[172:175]\n\t" \
  "ds_read_b64_tr_b16 v[78:79], %[w0] offset:2464\n\t" \
  "s_waitcnt lgkmcnt(15)\n\t" \
  "v_mfma_f32_16x16x32_bf16 v[176:179], v[116:119], v[48:51], v[176:179]\n\t" \
  "ds_read_b64_tr_b16 v[80:81], %[w0] offset:192\n\t" \
  "v_mfma_f32_16x16x32_bf16 v[180:183], v[116:119], v[52:55], v[180:183]\n\t" \
  "ds_read_b64_tr_b16 v[82:83], %[w0] offset:2496\n\t" \
  "s_waitcnt lgkmcnt(15)\n\t" \
  "v_mfma_f32_16x16x32_bf16 v[184:187], v[120:123], v[48:51], v[184:187]\n\t" \
  "ds_read_b64_tr_b16 v[84:85], %[w0] offset:224\n\t" \
  "v_mfma_f32_16x16x32_bf16 v[188:191], v[120:123], v[52:55], v[188:191]\n\t" \
  "ds_read_b64_tr_b16 v[86:87], %[w0] offset:2528\n\t" \
  "s_waitcnt lgkmcnt(15)\n\t" \
  "v_mfma_f32_16x16x32_bf16 v[192:195], v[124:127], v[48:51], v[192:195]\n\t" \
  "ds_read_b64_tr_b16 v[88:89], %[w0] offset:256\n\t" \
  "v_mfma_f32_16x16x32_bf16 v[196:199], v[124:127], v[52:55], v[196:199]\n\t" \
  "ds_read_b64_tr_b16 v[90:91], %[w0] offset:2560\n\t" \
  "s_cbranch_scc0 1b\n\t" \
  "s_waitcnt lgkmcnt(0)\n\t" \
  "s_nop 7\n\t" \
  "s_nop 7\n\t"
#define GA_CLOB "v40","v41","v42","v43","v44","v45","v46","v47","v48","v49","v50","v51","v52","v53","v54","v55","v56","v57","v58","v59","v60","v61","v62","v63","v64","v65","v66","v67","v68","v69","v70","v71","v72","v73","v74","v75","v76","v77","v78","v79","v80","v81","v82","v83","v84","v85","v86","v87","v88","v89","v90","v91","v92","v93","v94","v95","v96","v97","v98","v99","v100","v101","v102","v103","v104","v105","v106","v107","v108","v109","v110","v111","v112","v113","v114","v115","v116","v117","v118","v119","v120","v121","v122","v123","v124","v125","v126","v127"
#define GD_ASM \
  "ds_read_b64_tr_b16 v[88:89], %[wa0] offset:0\n\t" \
  "ds_read_b64_tr_b16 v[90:91], %[wa0] offset:4096\n\t" \
  "ds_read_b64_tr_b16 v[92:93], %[wa1] offset:0\n\t" \
  "ds_read_b64_tr_b16 v[94:95], %[wa1] offset:4096\n\t" \
  "ds_read_b128 v[40:43], %[fa] offset:0\n\t" \
  "ds_read_b128 v[44:47], %[fa] offset:1024\n\t" \
  "ds_read_b128 v[48:51], %[fa] offset:2048\n\t" \
  "ds_read_b128 v[52:55], %[fa] offset:3072\n\t" \
  "ds_read_b128 v[56:59], %[fa] offset:4096\n\t" \
  "ds_read_b128 v[60:63], %[fa] offset:5120\n\t" \
  "1:\n\t" \
  "s_waitcnt lgkmcnt(4)\n\t" \
  "v_mfma_f32_16x16x32_bf16 v[128:131], v[88:91], v[40:43], v[128:131]\n\t" \
  "ds_read_b64_tr_b16 v[96:97], %[wa0] offset:8192\n\t" \
  "v_mfma_f32_16x16x32_bf16 v[132:135], v[92:95], v[40:43], v[132:135]\n\t" \
  "ds_read_b64_tr_b16 v[98:99], %[wa0] offset:12288\n\t" \
  "v_mfma_f32_16x16x32_bf16 v[136:139], v[88:91], v[44:47], v[136:139]\n\t" \
  "ds_read_b64_tr_b16 v[100:101], %[wa1] offset:8192\n\t" \
  "v_mfma_f32_16x16x32_bf16 v[140:143], v[92:95], v[44:47], v[140:143]\n\t" \
  "ds_read_b64_tr_b16 v[102:103], %[wa1] offset:12288\n\t" \
  "s_waitcnt lgkmcnt(6)\n\t" \
  "v_mfma_f32_16x16x32_bf16 v[144:147], v[88:91], v[48:51], v[144:147]\n\t" \
  "ds_read_b128 v[64:67], %[fa] offset:6144\n\t" \
  "v_mfma_f32_16x16x32_bf16 v[148:151], v[92:95], v[48:51], v[148:151]\n\t" \
  "ds_read_b128 v[68:71], %[fa] offset:7168\n\t" \
  "v_mfma_f32_16x16x32_bf16 v[152:155], v[88:91], v[52:55], v[152:155]\n\t" \
  "ds_read_b128 v[72:75], %[fa] offset:8192\n\t" \
  "v_mfma_f32_16x16x32_bf16 v[156:159], v[92:95], v[52:55], v[156:159]\n\t" \
  "ds_read_b128 v[76:79], %[fa] offset:9216\n\t" \
  "s_waitcnt lgkmcnt(8)\n\t" \
  "v_mfma_f32_16x16x32_bf16 v[160:163], v[88:91], v[56:59], v[160:163]\n\t" \
  "ds_read_b128 v[80:83], %[fa] offset:10240\n\t" \
  "v_mfma_f32_16x16x32_bf16 v[164:167], v[92:95], v[56:59], v[164:167]\n\t" \
  "ds_read_b128 v[84:87], %[fa] offset:11264\n\t" \
  "v_mfma_f32_16x16x32_bf16 v[168:171], v[88:91], v[60:63], v[168:171]\n\t" \
  "s_barrier\n\t" \
  "v_mfma_f32_16x16x32_bf16 v[172:175], v[92:95], v[60:63], v[172:175]\n\t" \
  "s_waitcnt lgkmcnt(4)\n\t" \
  "v_mfma_f32_16x16x32_bf16 v[128:131], v[96:99], v[64:67], v[128:131]\n\t" \
  "ds_read_b64_tr_b16 v[88:89], %[wa0] offset:16384\n\t" \
  "v_mfma_f32_16x16x32_bf16 v[132:135], v[100:103], v[64:67], v[132:135]\n\t" \
  "ds_read_b64_tr_b16 v[90:91], %[wa0] offset:20480\n\t" \
  "v_mfma_f32_16x16x32_bf16 v[136:139], v[96:99], v[68:71], v[136:139]\n\t" \
  "ds_read_b64_tr_b16 v[92:93], %[wa1] offset:16384\n\t" \
  "v_mfma_f32_16x16x32_bf16 v[140:143], v[100:103], v[68:71], v[140:143]\n\t" \
  "ds_read_b64_tr_b16 v[94:95], %[wa1] offset:20480\n\t" \
  "s_waitcnt lgkmcnt(6)\n\t" \
  "v_mfma_f32_16x16x32_bf16 v[144:147], v[96:99], v[72:75], v[144:147]\n\t" \
  "ds_read_b128 v[40:43], %[fa] offset:12288\n\t" \
  "v_mfma_f32_16x16x32_bf16 v[148:151], v[100:103], v[72:75], v[148:151]\n\t" \
  "ds_read_b128 v[44:47], %[fa] offset:13312\n\t" \
  "v_mfma_f32_16x16x32_bf16 v[152:155], v[96:99], v[76:79], v[152:155]\n\t" \
  "ds_read_b128 v[48:51], %[fa] offset:14336\n\t" \
  "v_mfma_f32_16x16x32_bf16 v[156:159], v[100:103], v[76:79], v[156:159]\n\t" \
  "ds_read_b128 v[52:55], %[fa] offset:15360\n\t" \
  "s_waitcnt lgkmcnt(8)\n\t" \
  "v_mfma_f32_16x16x32_bf16 v[160:163], v[96:99], v[80:83], v[160:163]\n\t" \
  "ds_read_b128 v[56:59], %[fa] offset:16384\n\t" \
  "v_mfma_f32_16x16x32_bf16 v[164:167], v[100:103], v[80:83], v[164:167]\n\t" \
  "ds_read_b128 v[60:63], %[fa] offset:17408\n\t" \
  "v_mfma_f32_16x16x32_bf16 v[168:171], v[96:99], v[84:87], v[168:171]\n\t" \
  "s_barrier\n\t" \
  "v_mfma_f32_16x16x32_bf16 v[172:175], v[100:103], v[84:87], v[172:175]\n\t" \
  "s_waitcnt lgkmcnt(4)\n\t" \
  "v_mfma_f32_16x16x32_bf16 v[128:131], v[88:91], v[40:43], v[128:131]\n\t" \
  "ds_read_b64_tr_b16 v[96:97], %[wa0] offset:24576\n\t" \
  "v_mfma_f32_16x16x32_bf16 v[132:135], v[92:95], v[40:43], v[132:135]\n\t" \
  "ds_read_b64_tr_b16 v[98:99], %[wa0] offset:28672\n\t" \
  "s_add_u32 %[sw], %[sw], 32768\n\t" \
  "v_mfma_f32_16x16x32_bf16 v[136:139], v[88:91], v[44:47], v[136:139]\n\t" \
  "ds_read_b64_tr_b16 v[100:101], %[wa1] offset:24576\n\t" \
  "s_cmp_eq_u32 %[sw], 98304\n\t" \
  "v_mfma_f32_16x16x32_bf16 v[140:143], v[92:95], v[44:47], v[140:143]\n\t" \
  "ds_read_b64_tr_b16 v[102:103], %[wa1] offset:28672\n\t" \
  "s_cselect_b32 %[sw], 0, %[sw]\n\t" \
  "s_waitcnt lgkmcnt(6)\n\t" \
  "v_mfma_f32_16x16x32_bf16 v[144:147], v[88:91], v[48:51], v[144:147]\n\t" \
  "ds_read_b128 v[64:67], %[fa] offset:18432\n\t" \
  "v_mfma_f32_16x16x32_bf16 v[148:151], v[92:95], v[48:51], v[148:151]\n\t" \
  "ds_read_b128 v[68:71], %[fa] offset:19456\n\t" \
  "v_add_u32 %[wa0], %[sw], %[wb0]\n\t" \
  "v_mfma_f32_16x16x32_bf16 v[152:155], v[88:91], v[52:55], v[152:155]\n\t" \
  "ds_read_b128 v[72:75], %[fa] offset:20480\n\t" \
  "v_add_u32 %[wa1], %[sw], %[wb1]\n\t" \
  "v_mfma_f32_16x16x32_bf16 v[156:159], v[92:95], v[52:55], v[156:159]\n\t" \
  "ds_read_b128 v[76:79], %[fa] offset:21504\n\t" \
  "s_waitcnt lgkmcnt(8)\n\t" \
  "v_mfma_f32_16x16x32_bf16 v[160:163], v[88:91], v[56:59], v[160:163]\n\t" \
  "ds_read_b128 v[80:83], %[fa] offset:22528\n\t" \
  "v_mfma_f32_16x16x32_bf16 v[164:167], v[92:95], v[56:59], v[164:167]\n\t" \
  "ds_read_b128 v[84:87], %[fa] offset:23552\n\t" \
  "v_mfma_f32_16x16x32_bf16 v[168:171], v[88:91], v[60:63], v[168:171]\n\t" \
  "s_barrier\n\t" \
  "v_mfma_f32_16x16x32_bf16 v[172:175], v[92:95], v[60:63], v[172:175]\n\t" \
  "s_waitcnt lgkmcnt(4)\n\t" \
  "v_mfma_f32_16x16x32_bf16 v[128:131], v[96:99], v[64:67], v[128:131]\n\t" \
  "ds_read_b64_tr_b16 v[88:89], %[wa0] offset:0\n\t" \
  "s_add_u32 %[sf], %[sf], 24576\n\t" \
  "s_cmp_eq_u32 %[sf], 49152\n\t" \
  "v_mfma_f32_16x16x32_bf16 v[132:135], v[100:103], v[64:67], v[132:135]\n\t" \
  "ds_read_b64_tr_b16 v[90:91], %[wa0] offset:4096\n\t" \
  "s_cselect_b32 %[sf], 0, %[sf]\n\t" \
  "v_mfma_f32_16x16x32_bf16 v[136:139], v[96:99], v[68:71], v[136:139]\n\t" \
  "ds_read_b64_tr_b16 v[92:93], %[wa1] offset:0\n\t" \
  "v_add_u32 %[fa], %[sf], %[fb]\n\t" \
  "v_mfma_f32_16x16x32_bf16 v[140:143], v[100:103], v[68:71], v[140:143]\n\t" \
  "ds_read_b64_tr_b16 v[94:95], %[wa1] offset:4096\n\t" \
  "s_waitcnt lgkmcnt(6)\n\t" \
  "v_mfma_f32_16x16x32_bf16 v[144:147], v[96:99], v[72:75], v[144:147]\n\t" \
  "ds_read_b128 v[40:43], %[fa] offset:0\n\t" \
  "v_mfma_f32_16x16x32_bf16 v[148:151], v[100:103], v[72:75], v[148:151]\n\t" \
  "ds_read_b128 v[44:47], %[fa] offset:1024\n\t" \
  "v_mfma_f32_16x16x32_bf16 v[152:155], v[96:99], v[76:79], v[152:155]\n\t" \
  "ds_read_b128 v[48:51], %[fa] offset:2048\n\t" \
  "s_add_u32 %[it], %[it], 1\n\t" \
  "v_mfma_f32_16x16x32_bf16 v[156:159], v[100:103], v[76:79], v[156:159]\n\t" \
  "ds_read_b128 v[52:55], %[fa] offset:3072\n\t" \
  "s_waitcnt lgkmcnt(8)\n\t" \
  "v_mfma_f32_16x16x32_bf16 v[160:163], v[96:99], v[80:83], v[160:163]\n\t" \
  "ds_read_b128 v[56:59], %[fa] offset:4096\n\t" \
  "v_mfma_f32_16x16x32_bf16 v[164:167], v[100:103], v[80:83], v[164:167]\n\t" \
  "ds_read_b128 v[60:63], %[fa] offset:5120\n\t" \
  "v_mfma_f32_16x16x32_bf16 v[168:171], v[96:99], v[84:87], v[168:171]\n\t" \
  "s_barrier\n\t" \
  "v_mfma_f32_16x16x32_bf16 v[172:175], v[100:103], v[84:87], v[172:175]\n\t" \
  "s_cbranch_scc0 1b\n\t" \
  "s_waitcnt lgkmcnt(0)\n\t" \
  "s_nop 7\n\t" \
  "s_nop 7\n\t"
#define GD_CLOB "v40","v41","v42","v43","v44","v45","v46","v47","v48","v49","v50","v51","v52","v53","v54","v55","v56","v57","v58","v59","v60","v61","v62","v63","v64","v65","v66","v67","v68","v69","v70","v71","v72","v73","v74","v75","v76","v77","v78","v79","v80","v81","v82","v83","v84","v85","v86","v87","v88","v89","v90","v91","v92","v93","v94","v95","v96","v97","v98","v99","v100","v101","v102","v103"
#define GE_ASM \
  "ds_read_b64_tr_b16 v[112:113], %[wa0] offset:0\n\t" \
  "ds_read_b64_tr_b16 v[114:115], %[wa0] offset:4096\n\t" \
  "ds_read_b64_tr_b16 v[116:117], %[wa1] offset:0\n\t" \
  "ds_read_b64_tr_b16 v[118:119], %[wa1] offset:4096\n\t" \
  "ds_read_b128 v[40:43], %[fa] offset:0\n\t" \
  "ds_read_b128 v[44:47], %[fa] offset:1024\n\t" \
  "ds_read_b128 v[48:51], %[fa] offset:2048\n\t" \
  "ds_read_b128 v[52:55], %[fa] offset:3072\n\t" \
  "ds_read_b128 v[56:59], %[fa] offset:4096\n\t" \
  "ds_read_b128 v[60:63], %[fa] offset:5120\n\t" \
  "ds_read_b128 v[64:67], %[fa] offset:6144\n\t" \
  "ds_read_b128 v[68:71], %[fa] offset:7168\n\t" \
  "ds_read_b128 v[72:75], %[fa] offset:8192\n\t" \
  "1:\n\t" \
  "s_waitcnt lgkmcnt(7)\n\t" \
  "v_mfma_f32_16x16x32_bf16 v[128:131], v[40:43], v[112:115], v[128:131]\n\t" \
  "ds_read_b64_tr_b16 v[120:121], %[wa0] offset:8192\n\t" \
  "v_mfma_f32_16x16x32_bf16 v[132:135], v[40:43], v[116:119], v[132:135]\n\t" \
  "ds_read_b64_tr_b16 v[122:123], %[wa0] offset:12288\n\t" \
  "v_mfma_f32_16x16x32_bf16 v[136:139], v[44:47], v[112:115], v[136:139]\n\t" \
  "ds_read_b64_tr_b16 v[124:125], %[wa1] offset:8192\n\t" \
  "v_mfma_f32_16x16x32_bf16 v[140:143], v[44:47], v[116:119], v[140:143]\n\t" \
  "ds_read_b64_tr_b16 v[126:127], %[wa1] offset:12288\n\t" \
  "s_waitcnt lgkmcnt(9)\n\t" \
  "v_mfma_f32_16x16x32_bf16 v[144:147], v[48:51], v[112:115], v[144:147]\n\t" \
  "ds_read_b128 v[76:79], %[fa] offset:9216\n\t" \
  "v_mfma_f32_16x16x32_bf16 v[148:151], v[48:51], v[116:119], v[148:151]\n\t" \
  "ds_read_b128 v[80:83], %[fa] offset:10240\n\t" \
  "v_mfma_f32_16x16x32_bf16 v[152:155], v[52:55], v[112:115], v[152:155]\n\t" \
  "ds_read_b128 v[84:87], %[fa] offset:11264\n\t" \
  "v_mfma_f32_16x16x32_bf16 v[156:159], v[52:55], v[116:119], v[156:159]\n\t" \
  "ds_read_b128 v[88:91], %[fa] offset:12288\n\t" \
  "s_waitcnt lgkmcnt(11)\n\t" \
  "v_mfma_f32_16x16x32_bf16 v[160:163], v[56:59], v[112:115], v[160:163]\n\t" \
  "ds_read_b128 v[92:95], %[fa] offset:13312\n\t" \
  "v_mfma_f32_16x16x32_bf16 v[164:167], v[56:59], v[116:119], v[164:167]\n\t" \
  "ds_read_b128 v[96:99], %[fa] offset:14336\n\t" \
  "v_mfma_f32_16x16x32_bf16 v[168:171], v[60:63], v[112:115], v[168:171]\n\t" \
  "ds_read_b128 v[100:103], %[fa] offset:15360\n\t" \
  "v_mfma_f32_16x16x32_bf16 v[172:175], v[60:63], v[116:119], v[172:175]\n\t" \
  "ds_read_b128 v[104:107], %[fa] offset:16384\n\t" \
  "s_waitcnt lgkmcnt(13)\n\t" \
  "v_mfma_f32_16x16x32_bf16 v[176:179], v[64:67], v[112:115], v[176:179]\n\t" \
  "ds_read_b128 v[108:111], %[fa] offset:17408\n\t" \
  "v_mfma_f32_16x16x32_bf16 v[180:183], v[64:67], v[116:119], v[180:183]\n\t" \
  "v_mfma_f32_16x16x32_bf16 v[184:187], v[68:71], v[112:115], v[184:187]\n\t" \
  "v_mfma_f32_16x16x32_bf16 v[188:191], v[68:71], v[116:119], v[188:191]\n\t" \
  "s_waitcnt lgkmcnt(13)\n\t" \
  "v_mfma_f32_16x16x32_bf16 v[192:195], v[72:75], v[112:115], v[192:195]\n\t" \
  "s_barrier\n\t" \
  "v_mfma_f32_16x16x32_bf16 v[196:199], v[72:75], v[116:119], v[196:199]\n\t" \
  "s_waitcnt lgkmcnt(7)\n\t" \
  "v_mfma_f32_16x16x32_bf16 v[128:131], v[76:79], v[120:123], v[128:131]\n\t" \
  "ds_read_b64_tr_b16 v[112:113], %[wa0] offset:16384\n\t" \
  "v_mfma_f32_16x16x32_bf16 v[132:135], v[76:79], v[124:127], v[132:135]\n\t" \
  "ds_read_b64_tr_b16 v[114:115], %[wa0] offset:20480\n\t" \
  "v_mfma_f32_16x16x32_bf16 v[136:139], v[80:83], v[120:123], v[136:139]\n\t" \
  "ds_read_b64_tr_b16 v[116:117], %[wa1] offset:16384\n\t" \
  "v_mfma_f32_16x16x32_bf16 v[140:143], v[80:83], v[124:127], v[140:143]\n\t" \
  "ds_read_b64_tr_b16 v[118:119], %[wa1] offset:20480\n\t" \
  "s_waitcnt lgkmcnt(9)\n\t" \
  "v_mfma_f32_16x16x32_bf16 v[144:147], v[84:87], v[120:123], v[144:147]\n\t" \
  "ds_read_b128 v[40:43], %[fa] offset:18432\n\t" \
  "v_mfma_f32_16x16x32_bf16 v[148:151], v[84:87], v[124:127], v[148:151]\n\t" \
  "ds_read_b128 v[44:47], %[fa] offset:19456\n\t" \
  "v_mfma_f32_16x16x32_bf16 v[152:155], v[88:91], v[120:123], v[152:155]\n\t" \
  "ds_read_b128 v[48:51], %[fa] offset:20480\n\t" \
  "v_mfma_f32_16x16x32_bf16 v[156:159], v[88:91], v[124:127], v[156:159]\n\t" \
  "ds_read_b128 v[52:55], %[fa] offset:21504\n\t" \
  "s_waitcnt lgkmcnt(11)\n\t" \
  "v_mfma_f32_16x16x32_bf16 v[160:163], v[92:95], v[120:123], v[160:163]\n\t" \
  "ds_read_b128 v[56:59], %[fa] offset:22528\n\t" \
  "v_mfma_f32_16x16x32_bf16 v[164:167], v[92:95], v[124:127], v[164:167]\n\t" \
  "ds_read_b128 v[60:63], %[fa] offset:23552\n\t" \
  "v_mfma_f32_16x16x32_bf16 v[168:171], v[96:99], v[120:123], v[168:171]\n\t" \
  "ds_read_b128 v[64:67], %[fa] offset:24576\n\t" \
  "v_mfma_f32_16x16x32_bf16 v[172:175], v[96:99], v[124:127], v[172:175]\n\t" \
  "ds_read_b128 v[68:71], %[fa] offset:25600\n\t" \
  "s_waitcnt lgkmcnt(13)\n\t" \
  "v_mfma_f32_16x16x32_bf16 v[176:179], v[100:103], v[120:123], v[176:179]\n\t" \
  "ds_read_b128 v[72:75], %[fa] offset:26624\n\t" \
  "v_mfma_f32_16x16x32_bf16 v[180:183], v[100:103], v[124:127], v[180:183]\n\t" \
  "v_mfma_f32_16x16x32_bf16 v[184:187], v[104:107], v[120:123], v[184:187]\n\t" \
  "v_mfma_f32_16x16x32_bf16 v[188:191], v[104:107], v[124:127], v[188:191]\n\t" \
  "s_waitcnt lgkmcnt(13)\n\t" \
  "v_mfma_f32_16x16x32_bf16 v[192:195], v[108:111], v[120:123], v[192:195]\n\t" \
  "s_barrier\n\t" \
  "v_mfma_f32_16x16x32_bf16 v[196:199], v[108:111], v[124:127], v[196:199]\n\t" \
  "s_waitcnt lgkmcnt(7)\n\t" \
  "v_mfma_f32_16x16x32_bf16 v[128:131], v[40:43], v[112:115], v[128:131]\n\t" \
  "ds_read_b64_tr_b16 v[120:121], %[wa0] offset:24576\n\t" \
  "v_mfma_f32_16x16x32_bf16 v[132:135], v[40:43], v[116:119], v[132:135]\n\t" \
  "ds_read_b64_tr_b16 v[122:123], %[wa0] offset:28672\n\t" \
  "v_mfma_f32_16x16x32_bf16 v[136:139], v[44:47], v[112:115], v[136:139]\n\t" \
  "ds_read_b64_tr_b16 v[124:125], %[wa1] offset:24576\n\t" \
  "v_mfma_f32_16x16x32_bf16 v[140:143], v[44:47], v[116:119], v[140:143]\n\t" \
  "ds_read_b64_tr_b16 v[126:127], %[wa1] offset:28672\n\t" \
  "s_waitcnt lgkmcnt(9)\n\t" \
  "v_mfma_f32_16x16x32_bf16 v[144:147], v[48:51], v[112:115], v[144:147]\n\t" \
  "ds_read_b128 v[76:79], %[fa] offset:27648\n\t" \
  "v_mfma_f32_16x16x32_bf16 v[148:151], v[48:51], v[116:119], v[148:151]\n\t" \
  "ds_read_b128 v[80:83], %[fa] offset:28672\n\t" \
  "v_mfma_f32_16x16x32_bf16 v[152:155], v[52:55], v[112:115], v[152:155]\n\t" \
  "ds_read_b128 v[84:87], %[fa] offset:29696\n\t" \
  "v_mfma_f32_16x16x32_bf16 v[156:159], v[52:55], v[116:119], v[156:159]\n\t" \
  "ds_read_b128 v[88:91], %[fa] offset:30720\n\t" \
  "s_waitcnt lgkmcnt(11)\n\t" \
  "v_mfma_f32_16x16x32_bf16 v[160:163], v[56:59], v[112:115], v[160:163]\n\t" \
  "ds_read_b128 v[92:95], %[fa] offset:31744\n\t" \
  "v_mfma_f32_16x16x32_bf16 v[164:167], v[56:59], v[116:119], v[164:167]\n\t" \
  "ds_read_b128 v[96:99], %[fa] offset:32768\n\t" \
  "v_mfma_f32_16x16x32_bf16 v[168:171], v[60:63], v[112:115], v[168:171]\n\t" \
  "ds_read_b128 v[100:103], %[fa] offset:33792\n\t" \
  "v_mfma_f32_16x16x32_bf16 v[172:175], v[60:63], v[116:119], v[172:175]\n\t" \
  "ds_read_b128 v[104:107], %[fa] offset:34816\n\t" \
  "s_waitcnt lgkmcnt(13)\n\t" \
  "v_mfma_f32_16x16x32_bf16 v[176:179], v[64:67], v[112:115], v[176:179]\n\t" \
  "ds_read_b128 v[108:111], %[fa] offset:35840\n\t" \
  "v_mfma_f32_16x16x32_bf16 v[180:183], v[64:67], v[116:119], v[180:183]\n\t" \
  "v_mfma_f32_16x16x32_bf16 v[184:187], v[68:71], v[112:115], v[184:187]\n\t" \
  "v_mfma_f32_16x16x32_bf16 v[188:191], v[68:71], v[116:119], v[188:191]\n\t" \
  "s_waitcnt lgkmcnt(13)\n\t" \
  "v_mfma_f32_16x16x32_bf16 v[192:195], v[72:75], v[112:115], v[192:195]\n\t" \
  "s_barrier\n\t" \
  "v_mfma_f32_16x16x32_bf16 v[196:199], v[72:75], v[116:119], v[196:199]\n\t" \
  "s_waitcnt lgkmcnt(7)\n\t" \
  "v_mfma_f32_16x16x32_bf16 v[128:131], v[76:79], v[120:123], v[128:131]\n\t" \
  "ds_read_b64_tr_b16 v[112:113], %[wa0] offset:32768\n\t" \
  "v_mfma_f32_16x16x32_bf16 v[132:135], v[76:79], v[124:127], v[132:135]\n\t" \
  "ds_read_b64_tr_b16 v[114:115], %[wa0] offset:36864\n\t" \
  "v_mfma_f32_16x16x32_bf16 v[136:139], v[80:83], v[120:123], v[136:139]\n\t" \
  "ds_read_b64_tr_b16 v[116:117], %[wa1] offset:32768\n\t" \
  "v_mfma_f32_16x16x32_bf16 v[140:143], v[80:83], v[124:127], v[140:143]\n\t" \
  "ds_read_b64_tr_b16 v[118:119], %[wa1] offset:36864\n\t" \
  "s_waitcnt lgkmcnt(9)\n\t" \
  "v_mfma_f32_16x16x32_bf16 v[144:147], v[84:87], v[120:123], v[144:147]\n\t" \
  "ds_read_b128 v[40:43], %[fa] offset:36864\n\t" \
  "v_mfma_f32_16x16x32_bf16 v[148:151], v[84:87], v[124:127], v[148:151]\n\t" \
  "ds_read_b128 v[44:47], %[fa] offset:37888\n\t" \
  "v_mfma_f32_16x16x32_bf16 v[152:155], v[88:91], v[120:123], v[152:155]\n\t" \
  "ds_read_b128 v[48:51], %[fa] offset:38912\n\t" \
  "v_mfma_f32_16x16x32_bf16 v[156:159], v[88:91], v[124:127], v[156:159]\n\t" \
  "ds_read_b128 v[52:55], %[fa] offset:39936\n\t" \
  "s_waitcnt lgkmcnt(11)\n\t" \
  "v_mfma_f32_16x16x32_bf16 v[160:163], v[92:95], v[120:123], v[160:163]\n\t" \
  "ds_read_b128 v[56:59], %[fa] offset:40960\n\t" \
  "v_mfma_f32_16x16x32_bf16 v[164:167], v[92:95], v[124:127], v[164:167]\n\t" \
  "ds_read_b128 v[60:63], %[fa] offset:41984\n\t" \
  "v_mfma_f32_16x16x32_bf16 v[168:171], v[96:99], v[120:123], v[168:171]\n\t" \
  "ds_read_b128 v[64:67], %[fa] offset:43008\n\t" \
  "v_mfma_f32_16x16x32_bf16 v[172:175], v[96:99], v[124:127], v[172:175]\n\t" \
  "ds_read_b128 v[68:71], %[fa] offset:44032\n\t" \
  "s_waitcnt lgkmcnt(13)\n\t" \
  "v_mfma_f32_16x16x32_bf16 v[176:179], v[100:103], v[120:123], v[176:179]\n\t" \
  "ds_read_b128 v[72:75], %[fa] offset:45056\n\t" \
  "v_mfma_f32_16x16x32_bf16 v[180:183], v[100:103], v[124:127], v[180:183]\n\t" \
  "v_mfma_f32_16x16x32_bf16 v[184:187], v[104:107], v[120:123], v[184:187]\n\t" \
  "v_mfma_f32_16x16x32_bf16 v[188:191], v[104:107], v[124:127], v[188:191]\n\t" \
  "s_waitcnt lgkmcnt(13)\n\t" \
  "v_mfma_f32_16x16x32_bf16 v[192:195], v[108:111], v[120:123], v[192:195]\n\t" \
  "s_barrier\n\t" \
  "v_mfma_f32_16x16x32_bf16 v[196:199], v[108:111], v[124:127], v[196:199]\n\t" \
  "s_waitcnt lgkmcnt(7)\n\t" \
  "v_mfma_f32_16x16x32_bf16 v[128:131], v[40:43], v[112:115], v[128:131]\n\t" \
  "ds_read_b64_tr_b16 v[120:121], %[wa0] offset:40960\n\t" \
  "v_mfma_f32_16x16x32_bf16 v[132:135], v[40:43], v[116:119], v[132:135]\n\t" \
  "ds_read_b64_tr_b16 v[122:123], %[wa0] offset:45056\n\t" \
  "s_add_u32 %[sw], %[sw], 49152\n\t" \
  "v_mfma_f32_16x16x32_bf16 v[136:139], v[44:47], v[112:115], v[136:139]\n\t" \
  "ds_read_b64_tr_b16 v[124:125], %[wa1] offset:40960\n\t" \
  "s_cmp_eq_u32 %[sw], 98304\n\t" \
  "v_mfma_f32_16x16x32_bf16 v[140:143], v[44:47], v[116:119], v[140:143]\n\t" \
  "ds_read_b64_tr_b16 v[126:127], %[wa1] offset:45056\n\t" \
  "s_cselect_b32 %[sw], 0, %[sw]\n\t" \
  "s_waitcnt lgkmcnt(9)\n\t" \
  "v_mfma_f32_16x16x32_bf16 v[144:147], v[48:51], v[112:115], v[144:147]\n\t" \
  "ds_read_b128 v[76:79], %[fa] offset:46080\n\t" \
  "v_mfma_f32_16x16x32_bf16 v[148:151], v[48:51], v[116:119], v[148:151]\n\t" \
  "ds_read_b128 v[80:83], %[fa] offset:47104\n\t" \
  "v_add_u32 %[wa0], %[sw], %[wb0]\n\t" \
  "v_mfma_f32_16x16x32_bf16 v[152:155], v[52:55], v[112:115], v[152:155]\n\t" \
  "ds_read_b128 v[84:87], %[fa] offset:48128\n\t" \
  "v_add_u32 %[wa1], %[sw], %[wb1]\n\t" \
  "v_mfma_f32_16x16x32_bf16 v[156:159], v[52:55], v[116:119], v[156:159]\n\t" \
  "ds_read_b128 v[88:91], %[fa] offset:49152\n\t" \
  "s_waitcnt lgkmcnt(11)\n\t" \
  "v_mfma_f32_16x16x32_bf16 v[160:163], v[56:59], v[112:115], v[160:163]\n\t" \
  "ds_read_b128 v[92:95], %[fa] offset:50176\n\t" \
  "v_mfma_f32_16x16x32_bf16 v[164:167], v[56:59], v[116:119], v[164:167]\n\t" \
  "ds_read_b128 v[96:99], %[fa] offset:51200\n\t" \
  "v_mfma_f32_16x16x32_bf16 v[168:171], v[60:63], v[112:115], v[168:171]\n\t" \
  "ds_read_b128 v[100:103], %[fa] offset:52224\n\t" \
  "v_mfma_f32_16x16x32_bf16 v[172:175], v[60:63], v[116:119], v[172:175]\n\t" \
  "ds_read_b128 v[104:107], %[fa] offset:53248\n\t" \
  "s_waitcnt lgkmcnt(13)\n\t" \
  "v_mfma_f32_16x16x32_bf16 v[176:179], v[64:67], v[112:115], v[176:179]\n\t" \
  "ds_read_b128 v[108:111], %[fa] offset:54272\n\t" \
  "v_mfma_f32_16x16x32_bf16 v[180:183], v[64:67], v[116:119], v[180:183]\n\t" \
  "v_mfma_f32_16x16x32_bf16 v[184:187], v[68:71], v[112:115], v[184:187]\n\t" \
  "v_mfma_f32_16x16x32_bf16 v[188:191], v[68:71], v[116:119], v[188:191]\n\t" \
  "s_waitcnt lgkmcnt(13)\n\t" \
  "v_mfma_f32_16x16x32_bf16 v[192:195], v[72:75], v[112:115], v[192:195]\n\t" \
  "s_barrier\n\t" \
  "v_mfma_f32_16x16x32_bf16 v[196:199], v[72:75], v[116:119], v[196:199]\n\t" \
  "s_waitcnt lgkmcnt(7)\n\t" \
  "v_mfma_f32_16x16x32_bf16 v[128:131], v[76:79], v[120:123], v[128:131]\n\t" \
  "ds_read_b64_tr_b16 v[112:113], %[wa0] offset:0\n\t" \
  "v_mfma_f32_16x16x32_bf16 v[132:135], v[76:79], v[124:127], v[132:135]\n\t" \
  "ds_read_b64_tr_b16 v[114:115], %[wa0] offset:4096\n\t" \
  "v_mfma_f32_16x16x32_bf16 v[136:139], v[80:83], v[120:123], v[136:139]\n\t" \
  "ds_read_b64_tr_b16 v[116:117], %[wa1] offset:0\n\t" \
  "v_mfma_f32_16x16x32_bf16 v[140:143], v[80:83], v[124:127], v[140:143]\n\t" \
  "ds_read_b64_tr_b16 v[118:119], %[wa1] offset:4096\n\t" \
  "s_waitcnt lgkmcnt(9)\n\t" \
  "v_mfma_f32_16x16x32_bf16 v[144:147], v[84:87], v[120:123], v[144:147]\n\t" \
  "ds_read_b128 v[40:43], %[fa] offset:0\n\t" \
  "v_mfma_f32_16x16x32_bf16 v[148:151], v[84:87], v[124:127], v[148:151]\n\t" \
  "ds_read_b128 v[44:47], %[fa] offset:1024\n\t" \
  "v_mfma_f32_16x16x32_bf16 v[152:155], v[88:91], v[120:123], v[152:155]\n\t" \
  "ds_read_b128 v[48:51], %[fa] offset:2048\n\t" \
  "s_add_u32 %[it], %[it], 1\n\t" \
  "v_mfma_f32_16x16x32_bf16 v[156:159], v[88:91], v[124:127], v[156:159]\n\t" \
  "ds_read_b128 v[52:55], %[fa] offset:3072\n\t" \
  "s_waitcnt lgkmcnt(11)\n\t" \
  "v_mfma_f32_16x16x32_bf16 v[160:163], v[92:95], v[120:123], v[160:163]\n\t" \
  "ds_read_b128 v[56:59], %[fa] offset:4096\n\t" \
  "v_mfma_f32_16x16x32_bf16 v[164:167], v[92:95], v[124:127], v[164:167]\n\t" \
  "ds_read_b128 v[60:63], %[fa] offset:5120\n\t" \
  "v_mfma_f32_16x16x32_bf16 v[168:171], v[96:99], v[120:123], v[168:171]\n\t" \
  "ds_read_b128 v[64:67], %[fa] offset:6144\n\t" \
  "v_mfma_f32_16x16x32_bf16 v[172:175], v[96:99], v[124:127], v[172:175]\n\t" \
  "ds_read_b128 v[68:71], %[fa] offset:7168\n\t" \
  "s_waitcnt lgkmcnt(13)\n\t" \
  "v_mfma_f32_16x16x32_bf16 v[176:179], v[100:103], v[120:123], v[176:179]\n\t" \
  "ds_read_b128 v[72:75], %[fa] offset:8192\n\t" \
  "v_mfma_f32_16x16x32_bf16 v[180:183], v[100:103], v[124:127], v[180:183]\n\t" \
  "v_mfma_f32_16x16x32_bf16 v[184:187], v[104:107], v[120:123], v[184:187]\n\t" \
  "v_mfma_f32_16x16x32_bf16 v[188:191], v[104:107], v[124:127], v[188:191]\n\t" \
  "s_waitcnt lgkmcnt(13)\n\t" \
  "v_mfma_f32_16x16x32_bf16 v[192:195], v[108:111], v[120:123], v[192:195]\n\t" \
  "s_barrier\n\t" \
  "v_mfma_f32_16x16x32_bf16 v[196:199], v[108:111], v[124:127], v[196:199]\n\t" \
  "s_cbranch_scc0 1b\n\t" \
  "s_waitcnt lgkmcnt(0)\n\t" \
  "s_nop 7\n\t" \
  "s_nop 7\n\t"
#define GE_CLOB "v40","v41","v42","v43","v44","v45","v46","v47","v48","v49","v50","v51","v52","v53","v54","v55","v56","v57","v58","v59","v60","v61","v62","v63","v64","v65","v66","v67","v68","v69","v70","v71","v72","v73","v74","v75","v76","v77","v78","v79","v80","v81","v82","v83","v84","v85","v86","v87","v88","v89","v90","v91","v92","v93","v94","v95","v96","v97","v98","v99","v100","v101","v102","v103","v104","v105","v106","v107","v108","v109","v110","v111","v112","v113","v114","v115","v116","v117","v118","v119","v120","v121","v122","v123","v124","v125","v126","v127"
#define LWDP_ASM \
  "s_mov_b32 m0, %[lb]\n\t" \
  "s_mov_b32 %[t], %[np]\n\t" \
  "1:\n\t" \
  "buffer_load_dwordx4 %[v0], %[d], %[so] offen offset:0 lds\n\t" \
  "s_add_u32 %[lb], %[lb], %[ls]\n\t" \
  "s_cmp_ge_u32 %[lb], %[le]\n\t" \
  "buffer_load_dwordx4 %[v1], %[d], %[so] offen offset:1024 lds\n\t" \
  "s_cselect_b32 %[lb], %[l0], %[lb]\n\t" \
  "buffer_load_dwordx4 %[v2], %[d], %[so] offen offset:2048 lds\n\t" \
  "buffer_load_dwordx4 %[v3], %[d], %[so] offen offset:3072 lds\n\t" \
  "s_mov_b32 m0, %[lb]\n\t" \
  "s_add_u32 %[so], %[so], %[ks]\n\t" \
  "s_sleep %[sl]\n\t" \
  "s_add_u32 %[t], %[t], 1\n\t" \
  "s_cbranch_scc0 1b\n\t"
#define LWD_ASM \
  "s_mov_b32 m0, %[lb]\n\t" \
  "s_mov_b32 %[t], -9\n\t" \
  "1:\n\t" \
  "s_waitcnt vmcnt(36)\n\t" \
  "s_barrier\n\t" \
  "buffer_load_dwordx4 %[v0], %[d], %[so] offen offset:0 lds\n\t" \
  "s_add_u32 %[lb], %[lb], %[ls]\n\t" \
  "s_cmp_ge_u32 %[lb], %[le]\n\t" \
  "buffer_load_dwordx4 %[v1], %[d], %[so] offen offset:1024 lds\n\t" \
  "s_cselect_b32 %[lb], %[l0], %[lb]\n\t" \
  "buffer_load_dwordx4 %[v2], %[d], %[so] offen offset:2048 lds\n\t" \
  "buffer_load_dwordx4 %[v3], %[d], %[so] offen offset:3072 lds\n\t" \
  "s_mov_b32 m0, %[lb]\n\t" \
  "s_add_u32 %[so], %[so], %[ks]\n\t" \
  "s_add_u32 %[t], %[t], 1\n\t" \
  "s_cbranch_scc0 1b\n\t" \
  "s_waitcnt vmcnt(36)\n\t" \
  "s_barrier\n\t" \
  "s_mov_b32 %[t], -9\n\t" \
  "1:\n\t" \
  "s_waitcnt vmcnt(32)\n\t" \
  "s_barrier\n\t" \
  "buffer_load_dwordx4 %[v0], %[d], %[so] offen offset:0 lds\n\t" \
  "s_add_u32 %[lb], %[lb], %[ls]\n\t" \
  "s_cmp_ge_u32 %[lb], %[le]\n\t" \
  "buffer_load_dwordx4 %[v1], %[d], %[so] offen offset:1024 lds\n\t" \
  "s_cselect_b32 %[lb], %[l0], %[lb]\n\t" \
  "buffer_load_dwordx4 %[v2], %[d], %[so] offen offset:2048 lds\n\t" \
  "buffer_load_dwordx4 %[v3], %[d], %[so] offen offset:3072 lds\n\t" \
  "s_mov_b32 m0, %[lb]\n\t" \
  "s_add_u32 %[so], %[so], %[ks]\n\t" \
  "s_add_u32 %[t], %[t], 1\n\t" \
  "s_cbranch_scc0 1b\n\t" \
  "s_waitcnt vmcnt(32)\n\t" \
  "s_barrier\n\t" \
  "s_mov_b32 %[t], -9\n\t" \
  "1:\n\t" \
  "s_waitcnt vmcnt(28)\n\t" \
  "s_barrier\n\t" \
  "buffer_load_dwordx4 %[v0], %[d], %[so] offen offset:0 lds\n\t" \
  "s_add_u32 %[lb], %[lb], %[ls]\n\t" \
  "s_cmp_ge_u32 %[lb], %[le]\n\t" \
  "buffer_load_dwordx4 %[v1], %[d], %[so] offen offset:1024 lds\n\t" \
  "s_cselect_b32 %[lb], %[l0], %[lb]\n\t" \
  "buffer_load_dwordx4 %[v2], %[d], %[so] offen offset:2048 lds\n\t" \
  "buffer_load_dwordx4 %[v3], %[d], %[so] offen offset:3072 lds\n\t" \
  "s_mov_b32 m0, %[lb]\n\t" \
  "s_add_u32 %[so], %[so], %[ks]\n\t" \
  "s_add_u32 %[t], %[t], 1\n\t" \
  "s_cbranch_scc0 1b\n\t" \
  "s_waitcnt vmcnt(28)\n\t" \
  "s_barrier\n\t" \
  "s_mov_b32 %[t], -9\n\t" \
  "1:\n\t" \
  "s_waitcnt vmcnt(24)\n\t" \
  "s_barrier\n\t" \
  "buffer_load_dwordx4 %[v0], %[d], %[so] offen offset:0 lds\n\t" \
  "s_add_u32 %[lb], %[lb], %[ls]\n\t" \
  "s_cmp_ge_u32 %[lb], %[le]\n\t" \
  "buffer_load_dwordx4 %[v1], %[d], %[so] offen offset:1024 lds\n\t" \
  "s_cselect_b32 %[lb], %[l0], %[lb]\n\t" \
  "buffer_load_dwordx4 %[v2], %[d], %[so] offen offset:2048 lds\n\t" \
  "buffer_load_dwordx4 %[v3], %[d], %[so] offen offset:3072 lds\n\t" \
  "s_mov_b32 m0, %[lb]\n\t" \
  "s_add_u32 %[so], %[so], %[ks]\n\t" \
  "s_add_u32 %[t], %[t], 1\n\t" \
  "s_cbranch_scc0 1b\n\t" \
  "s_waitcnt vmcnt(24)\n\t" \
  "s_barrier\n\t" \
  "s_mov_b32 %[t], -9\n\t" \
  "1:\n\t" \
  "s_waitcnt vmcnt(20)\n\t" \
  "s_barrier\n\t" \
  "buffer_load_dwordx4 %[v0], %[d], %[so] offen offset:0 lds\n\t" \
  "s_add_u32 %[lb], %[lb], %[ls]\n\t" \
  "s_cmp_ge_u32 %[lb], %[le]\n\t" \
  "buffer_load_dwordx4 %[v1], %[d], %[so] offen offset:1024 lds\n\t" \
  "s_cselect_b32 %[lb], %[l0], %[lb]\n\t" \
  "buffer_load_dwordx4 %[v2], %[d], %[so] offen offset:2048 lds\n\t" \
  "buffer_load_dwordx4 %[v3], %[d], %[so] offen offset:3072 lds\n\t" \
  "s_mov_b32 m0, %[lb]\n\t" \
  "s_add_u32 %[so], %[so], %[ks]\n\t" \
  "s_add_u32 %[t], %[t], 1\n\t" \
  "s_cbranch_scc0 1b\n\t" \
  "s_waitcnt vmcnt(20)\n\t" \
  "s_barrier\n\t" \
  "s_mov_b32 %[t], -7\n\t" \
  "1:\n\t" \
  "s_waitcnt vmcnt(16)\n\t" \
  "s_barrier\n\t" \
  "buffer_load_dwordx4 %[v0], %[d], %[so] offen offset:0 lds\n\t" \
  "s_add_u32 %[lb], %[lb], %[ls]\n\t" \
  "s_cmp_ge_u32 %[lb], %[le]\n\t" \
  "buffer_load_dwordx4 %[v1], %[d], %[so] offen offset:1024 lds\n\t" \
  "s_cselect_b32 %[lb], %[l0], %[lb]\n\t" \
  "buffer_load_dwordx4 %[v2], %[d], %[so] offen offset:2048 lds\n\t" \
  "buffer_load_dwordx4 %[v3], %[d], %[so] offen offset:3072 lds\n\t" \
  "s_mov_b32 m0, %[lb]\n\t" \
  "s_add_u32 %[so], %[so], %[ks]\n\t" \
  "s_add_u32 %[t], %[t], 1\n\t" \
  "s_cbranch_scc0 1b\n\t" \
  "s_waitcnt vmcnt(16)\n\t" \
  "s_barrier\n\t" \
  "s_waitcnt vmcnt(12)\n\t" \
  "s_barrier\n\t" \
  "s_waitcnt vmcnt(8)\n\t" \
  "s_barrier\n\t" \
  "s_waitcnt vmcnt(4)\n\t" \
  "s_barrier\n\t" \
  "s_waitcnt vmcnt(0)\n\t" \
  "s_barrier\n\t" \
  "s_waitcnt vmcnt(0)\n\t" \
  "s_barrier\n\t" \
  "s_waitcnt vmcnt(0)\n\t" \
  "s_barrier\n\t"
#define LWEP_ASM \
  "s_mov_b32 m0, %[lb]\n\t" \
  "s_mov_b32 %[t], %[np]\n\t" \
  "1:\n\t" \
  "buffer_load_dwordx4 %[v0], %[d], %[so] offen offset:0 lds\n\t" \
  "s_add_u32 %[lb], %[lb], %[ls]\n\t" \
  "s_cmp_ge_u32 %[lb], %[le]\n\t" \
  "buffer_load_dwordx4 %[v1], %[d], %[so] offen offset:1024 lds\n\t" \
  "s_cselect_b32 %[lb], %[l0], %[lb]\n\t" \
  "buffer_load_dwordx4 %[v2], %[d], %[so] offen offset:2048 lds\n\t" \
  "buffer_load_dwordx4 %[v3], %[d], %[so] offen offset:3072 lds\n\t" \
  "s_mov_b32 m0, %[lb]\n\t" \
  "s_add_u32 %[so], %[so], %[ks]\n\t" \
  "s_sleep %[sl]\n\t" \
  "s_add_u32 %[t], %[t], 1\n\t" \
  "s_cbranch_scc0 1b\n\t"
#define LWE_ASM \
  "s_mov_b32 m0, %[lb]\n\t" \
  "s_mov_b32 %[t], -3\n\t" \
  "1:\n\t" \
  "s_waitcnt vmcnt(36)\n\t" \
  "s_barrier\n\t" \
  "buffer_load_dwordx4 %[v0], %[d], %[so] offen offset:0 lds\n\t" \
  "s_add_u32 %[lb], %[lb], %[ls]\n\t" \
  "s_cmp_ge_u32 %[lb], %[le]\n\t" \
  "buffer_load_dwordx4 %[v1], %[d], %[so] offen offset:1024 lds\n\t" \
  "s_cselect_b32 %[lb], %[l0], %[lb]\n\t" \
  "buffer_load_dwordx4 %[v2], %[d], %[so] offen offset:2048 lds\n\t" \
  "buffer_load_dwordx4 %[v3], %[d], %[so] offen offset:3072 lds\n\t" \
  "s_mov_b32 m0, %[lb]\n\t" \
  "s_add_u32 %[so], %[so], %[ks]\n\t" \
  "s_add_u32 %[t], %[t], 1\n\t" \
  "s_cbranch_scc0 1b\n\t" \
  "s_waitcnt vmcnt(36)\n\t" \
  "s_barrier\n\t" \
  "s_mov_b32 %[t], -3\n\t" \
  "1:\n\t" \
  "s_waitcnt vmcnt(32)\n\t" \
  "s_barrier\n\t" \
  "buffer_load_dwordx4 %[v0], %[d], %[so] offen offset:0 lds\n\t" \
  "s_add_u32 %[lb], %[lb], %[ls]\n\t" \
  "s_cmp_ge_u32 %[lb], %[le]\n\t" \
  "buffer_load_dwordx4 %[v1], %[d], %[so] offen offset:1024 lds\n\t" \
  "s_cselect_b32 %[lb], %[l0], %[lb]\n\t" \
  "buffer_load_dwordx4 %[v2], %[d], %[so] offen offset:2048 lds\n\t" \
  "buffer_load_dwordx4 %[v3], %[d], %[so] offen offset:3072 lds\n\t" \
  "s_mov_b32 m0, %[lb]\n\t" \
  "s_add_u32 %[so], %[so], %[ks]\n\t" \
  "s_add_u32 %[t], %[t], 1\n\t" \
  "s_cbranch_scc0 1b\n\t" \
  "s_waitcnt vmcnt(32)\n\t" \
  "s_barrier\n\t" \
  "s_mov_b32 %[t], -3\n\t" \
  "1:\n\t" \
  "s_waitcnt vmcnt(28)\n\t" \
  "s_barrier\n\t" \
  "buffer_load_dwordx4 %[v0], %[d], %[so] offen offset:0 lds\n\t" \
  "s_add_u32 %[lb], %[lb], %[ls]\n\t" \
  "s_cmp_ge_u32 %[lb], %[le]\n\t" \
  "buffer_load_dwordx4 %[v1], %[d], %[so] offen offset:1024 lds\n\t" \
  "s_cselect_b32 %[lb], %[l0], %[lb]\n\t" \
  "buffer_load_dwordx4 %[v2], %[d], %[so] offen offset:2048 lds\n\t" \
  "buffer_load_dwordx4 %[v3], %[d], %[so] offen offset:3072 lds\n\t" \
  "s_mov_b32 m0, %[lb]\n\t" \
  "s_add_u32 %[so], %[so], %[ks]\n\t" \
  "s_add_u32 %[t], %[t], 1\n\t" \
  "s_cbranch_scc0 1b\n\t" \
  "s_waitcnt vmcnt(28)\n\t" \
  "s_barrier\n\t" \
  "s_mov_b32 %[t], -3\n\t" \
  "1:\n\t" \
  "s_waitcnt vmcnt(24)\n\t" \
  "s_barrier\n\t" \
  "buffer_load_dwordx4 %[v0], %[d], %[so] offen offset:0 lds\n\t" \
  "s_add_u32 %[lb], %[lb], %[ls]\n\t" \
  "s_cmp_ge_u32 %[lb], %[le]\n\t" \
  "buffer_load_dwordx4 %[v1], %[d], %[so] offen offset:1024 lds\n\t" \
  "s_cselect_b32 %[lb], %[l0], %[lb]\n\t" \
  "buffer_load_dwordx4 %[v2], %[d], %[so] offen offset:2048 lds\n\t" \
  "buffer_load_dwordx4 %[v3], %[d], %[so] offen offset:3072 lds\n\t" \
  "s_mov_b32 m0, %[lb]\n\t" \
  "s_add_u32 %[so], %[so], %[ks]\n\t" \
  "s_add_u32 %[t], %[t], 1\n\t" \
  "s_cbranch_scc0 1b\n\t" \
  "s_waitcnt vmcnt(24)\n\t" \
  "s_barrier\n\t" \
  "s_mov_b32 %[t], -3\n\t" \
  "1:\n\t" \
  "s_waitcnt vmcnt(20)\n\t" \
  "s_barrier\n\t" \
  "buffer_load_dwordx4 %[v0], %[d], %[so] offen offset:0 lds\n\t" \
  "s_add_u32 %[lb], %[lb], %[ls]\n\t" \
  "s_cmp_ge_u32 %[lb], %[le]\n\t" \
  "buffer_load_dwordx4 %[v1], %[d], %[so] offen offset:1024 lds\n\t" \
  "s_cselect_b32 %[lb], %[l0], %[lb]\n\t" \
  "buffer_load_dwordx4 %[v2], %[d], %[so] offen offset:2048 lds\n\t" \
  "buffer_load_dwordx4 %[v3], %[d], %[so] offen offset:3072 lds\n\t" \
  "s_mov_b32 m0, %[lb]\n\t" \
  "s_add_u32 %[so], %[so], %[ks]\n\t" \
  "s_add_u32 %[t], %[t], 1\n\t" \
  "s_cbranch_scc0 1b\n\t" \
  "s_waitcnt vmcnt(20)\n\t" \
  "s_barrier\n\t" \
  "s_mov_b32 %[t], -3\n\t" \
  "1:\n\t" \
  "s_waitcnt vmcnt(16)\n\t" \
  "s_barrier\n\t" \
  "buffer_load_dwordx4 %[v0], %[d], %[so] offen offset:0 lds\n\t" \
  "s_add_u32 %[lb], %[lb], %[ls]\n\t" \
  "s_cmp_ge_u32 %[lb], %[le]\n\t" \
  "buffer_load_dwordx4 %[v1], %[d], %[so] offen offset:1024 lds\n\t" \
  "s_cselect_b32 %[lb], %[l0], %[lb]\n\t" \
  "buffer_load_dwordx4 %[v2], %[d], %[so] offen offset:2048 lds\n\t" \
  "buffer_load_dwordx4 %[v3], %[d], %[so] offen offset:3072 lds\n\t" \
  "s_mov_b32 m0, %[lb]\n\t" \
  "s_add_u32 %[so], %[so], %[ks]\n\t" \
  "s_add_u32 %[t], %[t], 1\n\t" \
  "s_cbranch_scc0 1b\n\t" \
  "s_waitcnt vmcnt(16)\n\t" \
  "s_barrier\n\t" \
  "s_mov_b32 %[t], -3\n\t" \
  "1:\n\t" \
  "s_waitcnt vmcnt(12)\n\t" \
  "s_barrier\n\t" \
  "buffer_load_dwordx4 %[v0], %[d], %[so] offen offset:0 lds\n\t" \
  "s_add_u32 %[lb], %[lb], %[ls]\n\t" \
  "s_cmp_ge_u32 %[lb], %[le]\n\t" \
  "buffer_load_dwordx4 %[v1], %[d], %[so] offen offset:1024 lds\n\t" \
  "s_cselect_b32 %[lb], %[l0], %[lb]\n\t" \
  "buffer_load_dwordx4 %[v2], %[d], %[so] offen offset:2048 lds\n\t" \
  "buffer_load_dwordx4 %[v3], %[d], %[so] offen offset:3072 lds\n\t" \
  "s_mov_b32 m0, %[lb]\n\t" \
  "s_add_u32 %[so], %[so], %[ks]\n\t" \
  "s_add_u32 %[t], %[t], 1\n\t" \
  "s_cbranch_scc0 1b\n\t" \
  "s_waitcnt vmcnt(12)\n\t" \
  "s_barrier\n\t" \
  "s_mov_b32 %[t], -3\n\t" \
  "1:\n\t" \
  "s_waitcnt vmcnt(8)\n\t" \
  "s_barrier\n\t" \
  "buffer_load_dwordx4 %[v0], %[d], %[so] offen offset:0 lds\n\t" \
  "s_add_u32 %[lb], %[lb], %[ls]\n\t" \
  "s_cmp_ge_u32 %[lb], %[le]\n\t" \
  "buffer_load_dwordx4 %[v1], %[d], %[so] offen offset:1024 lds\n\t" \
  "s_cselect_b32 %[lb], %[l0], %[lb]\n\t" \
  "buffer_load_dwordx4 %[v2], %[d], %[so] offen offset:2048 lds\n\t" \
  "buffer_load_dwordx4 %[v3], %[d], %[so] offen offset:3072 lds\n\t" \
  "s_mov_b32 m0, %[lb]\n\t" \
  "s_add_u32 %[so], %[so], %[ks]\n\t" \
  "s_add_u32 %[t], %[t], 1\n\t" \
  "s_cbranch_scc0 1b\n\t" \
  "s_mov_b32 m0, %[hl]\n\t" \
  "s_add_u32 %[t], %[hs], 4096\n\t" \
  "buffer_load_dwordx4 %[hv], %[hd], %[hs] offen offset:0 sc1 lds\n\t" \
  "buffer_load_dwordx4 %[hv], %[hd], %[hs] offen offset:1024 sc1 lds\n\t" \
  "buffer_load_dwordx4 %[hv], %[hd], %[hs] offen offset:2048 sc1 lds\n\t" \
  "buffer_load_dwordx4 %[hv], %[hd], %[hs] offen offset:3072 sc1 lds\n\t" \
  "s_add_u32 m0, m0, 4096\n\t" \
  "s_nop 0\n\t" \
  "buffer_load_dwordx4 %[hv], %[hd], %[t] offen sc1 lds\n\t" \
  "s_waitcnt vmcnt(13)\n\t" \
  "s_barrier\n\t" \
  "s_waitcnt vmcnt(9)\n\t" \
  "s_barrier\n\t" \
  "s_waitcnt vmcnt(5)\n\t" \
  "s_barrier\n\t" \
  "s_waitcnt vmcnt(5)\n\t" \
  "s_barrier\n\t" \
  "s_waitcnt vmcnt(0)\n\t" \
  "s_barrier\n\t"
#define LF3P_ASM \
  "s_mov_b32 m0, %[lb]\n\t" \
  "s_mov_b32 %[t], -8\n\t" \
  "1:\n\t" \
  "buffer_load_dwordx4 %[v0], %[d], %[so] offen offset:0 sc1 lds\n\t" \
  "s_add_u32 %[lb], %[lb], %[ls]\n\t" \
  "s_cmp_ge_u32 %[lb], %[le]\n\t" \
  "buffer_load_dwordx4 %[v0], %[d], %[so] offen offset:1024 sc1 lds\n\t" \
  "s_cselect_b32 %[lb], %[l0], %[lb]\n\t" \
  "buffer_load_dwordx4 %[v0], %[d], %[so] offen offset:2048 sc1 lds\n\t" \
  "s_mov_b32 m0, %[lb]\n\t" \
  "s_add_u32 %[so], %[so], %[ks]\n\t" \
  "s_add_u32 %[t], %[t], 1\n\t" \
  "s_cbranch_scc0 1b\n\t"
#define LF3_ASM \
  "s_mov_b32 m0, %[lb]\n\t" \
  "s_waitcnt vmcnt(18)\n\t" \
  "s_barrier\n\t" \
  "s_mov_b32 %[t], -56\n\t" \
  "1:\n\t" \
  "s_waitcnt vmcnt(15)\n\t" \
  "s_barrier\n\t" \
  "buffer_load_dwordx4 %[v0], %[d], %[so] offen offset:0 sc1 lds\n\t" \
  "s_add_u32 %[lb], %[lb], %[ls]\n\t" \
  "s_cmp_ge_u32 %[lb], %[le]\n\t" \
  "buffer_load_dwordx4 %[v0], %[d], %[so] offen offset:1024 sc1 lds\n\t" \
  "s_cselect_b32 %[lb], %[l0], %[lb]\n\t" \
  "buffer_load_dwordx4 %[v0], %[d], %[so] offen offset:2048 sc1 lds\n\t" \
  "s_mov_b32 m0, %[lb]\n\t" \
  "s_add_u32 %[so], %[so], %[ks]\n\t" \
  "s_add_u32 %[t], %[t], 1\n\t" \
  "s_cbranch_scc0 1b\n\t" \
  "s_waitcnt vmcnt(15)\n\t" \
  "s_barrier\n\t" \
  "s_waitcnt vmcnt(12)\n\t" \
  "s_barrier\n\t" \
  "s_waitcnt vmcnt(9)\n\t" \
  "s_barrier\n\t" \
  "s_waitcnt vmcnt(6)\n\t" \
  "s_barrier\n\t" \
  "s_waitcnt vmcnt(3)\n\t" \
  "s_barrier\n\t" \
  "s_waitcnt vmcnt(0)\n\t" \
  "s_barrier\n\t" \
  "s_waitcnt vmcnt(0)\n\t" \
  "s_barrier\n\t" \
  "s_waitcnt vmcnt(0)\n\t" \
  "s_barrier\n\t"
#define LF4P_ASM \
  "s_mov_b32 m0, %[lb]\n\t" \
  "s_mov_b32 %[t], -6\n\t" \
  "1:\n\t" \
  "buffer_load_dwordx4 %[v0], %[d], %[so] offen offset:0 sc1 lds\n\t" \
  "s_add_u32 %[lb], %[lb], %[ls]\n\t" \
  "s_cmp_ge_u32 %[lb], %[le]\n\t" \
  "buffer_load_dwordx4 %[v0], %[d], %[so] offen offset:1024 sc1 lds\n\t" \
  "s_cselect_b32 %[lb], %[l0], %[lb]\n\t" \
  "buffer_load_dwordx4 %[v0], %[d], %[so] offen offset:2048 sc1 lds\n\t" \
  "buffer_load_dwordx4 %[v0], %[d], %[so] offen offset:3072 sc1 lds\n\t" \
  "s_mov_b32 m0, %[lb]\n\t" \
  "s_add_u32 %[so], %[so], %[ks]\n\t" \
  "s_add_u32 %[t], %[t], 1\n\t" \
  "s_cbranch_scc0 1b\n\t"
#define LF4_ASM \
  "s_mov_b32 m0, %[lb]\n\t" \
  "s_waitcnt vmcnt(16)\n\t" \
  "s_barrier\n\t" \
  "s_mov_b32 %[t], -30\n\t" \
  "1:\n\t" \
  "s_waitcnt vmcnt(12)\n\t" \
  "s_barrier\n\t" \
  "buffer_load_dwordx4 %[v0], %[d], %[so] offen offset:0 sc1 lds\n\t" \
  "s_add_u32 %[lb], %[lb], %[ls]\n\t" \
  "s_cmp_ge_u32 %[lb], %[le]\n\t" \
  "buffer_load_dwordx4 %[v0], %[d], %[so] offen offset:1024 sc1 lds\n\t" \
  "s_cselect_b32 %[lb], %[l0], %[lb]\n\t" \
  "buffer_load_dwordx4 %[v0], %[d], %[so] offen offset:2048 sc1 lds\n\t" \
  "buffer_load_dwordx4 %[v0], %[d], %[so] offen offset:3072 sc1 lds\n\t" \
  "s_mov_b32 m0, %[lb]\n\t" \
  "s_add_u32 %[so], %[so], %[ks]\n\t" \
  "s_add_u32 %[t], %[t], 1\n\t" \
  "s_cbranch_scc0 1b\n\t" \
  "s_waitcnt vmcnt(12)\n\t" \
  "s_barrier\n\t" \
  "s_waitcnt vmcnt(8)\n\t" \
  "s_barrier\n\t" \
  "s_waitcnt vmcnt(4)\n\t" \
  "s_barrier\n\t" \
  "s_waitcnt vmcnt(0)\n\t" \
  "s_barrier\n\t" \
  "s_waitcnt vmcnt(0)\n\t" \
  "s_barrier\n\t" \
  "s_waitcnt vmcnt(0)\n\t" \
  "s_barrier\n\t"
#define LF5P_ASM \
  "s_mov_b32 m0, %[lb]\n\t" \
  "s_mov_b32 %[t], -6\n\t" \
  "1:\n\t" \
  "buffer_load_dwordx4 %[v0], %[d], %[so] offen offset:0 sc1 lds\n\t" \
  "s_add_u32 %[lb], %[lb], %[ls]\n\t" \
  "s_cmp_ge_u32 %[lb], %[le]\n\t" \
  "buffer_load_dwordx4 %[v0], %[d], %[so] offen offset:1024 sc1 lds\n\t" \
  "s_cselect_b32 %[lb], %[l0], %[lb]\n\t" \
  "buffer_load_dwordx4 %[v0], %[d], %[so] offen offset:2048 sc1 lds\n\t" \
  "buffer_load_dwordx4 %[v0], %[d], %[so] offen offset:3072 sc1 lds\n\t" \
  "s_add_u32 m0, m0, 4096\n\t" \
  "s_nop 0\n\t" \
  "buffer_load_dwordx4 %[v1], %[d], %[so] offen offset:0 sc1 lds\n\t" \
  "s_mov_b32 m0, %[lb]\n\t" \
  "s_add_u32 %[so], %[so], %[ks]\n\t" \
  "s_add_u32 %[t], %[t], 1\n\t" \
  "s_cbranch_scc0 1b\n\t"
#define LF5_ASM \
  "s_mov_b32 m0, %[lb]\n\t" \
  "s_waitcnt vmcnt(20)\n\t" \
  "s_barrier\n\t" \
  "s_mov_b32 %[t], -30\n\t" \
  "1:\n\t" \
  "s_waitcnt vmcnt(15)\n\t" \
  "s_barrier\n\t" \
  "buffer_load_dwordx4 %[v0], %[d], %[so] offen offset:0 sc1 lds\n\t" \
  "s_add_u32 %[lb], %[lb], %[ls]\n\t" \
  "s_cmp_ge_u32 %[lb], %[le]\n\t" \
  "buffer_load_dwordx4 %[v0], %[d], %[so] offen offset:1024 sc1 lds\n\t" \
  "s_cselect_b32 %[lb], %[l0], %[lb]\n\t" \
  "buffer_load_dwordx4 %[v0], %[d], %[so] offen offset:2048 sc1 lds\n\t" \
  "buffer_load_dwordx4 %[v0], %[d], %[so] offen offset:3072 sc1 lds\n\t" \
  "s_add_u32 m0, m0, 4096\n\t" \
  "s_nop 0\n\t" \
  "buffer_load_dwordx4 %[v1], %[d], %[so] offen offset:0 sc1 lds\n\t" \
  "s_mov_b32 m0, %[lb]\n\t" \
  "s_add_u32 %[so], %[so], %[ks]\n\t" \
  "s_add_u32 %[t], %[t], 1\n\t" \
  "s_cbranch_scc0 1b\n\t" \
  "s_waitcnt vmcnt(15)\n\t" \
  "s_barrier\n\t" \
  "s_waitcnt vmcnt(10)\n\t" \
  "s_barrier\n\t" \
  "s_waitcnt vmcnt(5)\n\t" \
  "s_barrier\n\t" \
  "s_waitcnt vmcnt(0)\n\t" \
  "s_barrier\n\t" \
  "s_waitcnt vmcnt(0)\n\t" \
  "s_barrier\n\t" \
  "s_waitcnt vmcnt(0)\n\t" \
  "s_barrier\n\t"
typedef float f32x8 __attribute__((ext_vector_type(8)));
typedef float f32x16 __attribute__((ext_vector_type(16)));
typedef float f32x32 __attribute__((ext_vector_type(32)));
template<class T> DI f32x4 sub4(T v,int i){ return (f32x4){v[4*i],v[4*i+1],v[4*i+2],v[4*i+3]}; }
constexpr int LA_ST=139264, LA_CS=LA_ST+1024, LA_O=34816;
DI int widA(int tb,int cb){ int hi=cb>=6; return (tb>>2)*2+hi+8*((tb&3)+4*(cb-6*hi)); }
DI void glds4(u32x4 d,int v0,int v1,int v2,int v3,int soff,int lds){
  asm volatile("s_mov_b32 m0, %0\n\ts_nop 0\n\tbuffer_load_dwordx4 %1, %5, %6 offen lds\n\tbuffer_load_dwordx4 %2, %5, %6 offen offset:1024 lds\n\t"
    "buffer_load_dwordx4 %3, %5, %6 offen offset:2048 lds\n\tbuffer_load_dwordx4 %4, %5, %6 offen offset:3072 lds"::"s"(lds),"v"(v0),"v"(v1),"v"(v2),"v"(v3),"s"(d),"s"(soff):"memory"); }
DI u32x2 mktag(u64 e){ asm volatile("":"+s"(e)); return __builtin_bit_cast(u32x2,e+0x9E3779B97F4A7C15ull); }
DI void phaseA(u32x4 x,u32x4 wqkv,u32x4 wsd,RS ws,int wid,int wave,int lane,u64 ep){
  const int xcd=wid&7,i5=wid>>3, tb=(xcd>>1)*4+(i5&3), cb=(xcd&1)*6+(i5>>2);
  const int g=lane>>4,tok=lane&15;
  if(__builtin_expect(wave>=4,1)){
    int rot=(i5&3)*2+(i5>>2)+(i5>>3); rot=rot>=9?rot-9:rot; int b=0;
    if(wave>=6){ const int j=wave-6, r=lane>>3; int v[4];
      UNR for(int i=0;i<4;i++) v[i]=(8*i+r)*1152+(((lane&7)^((4*i+(r>>1))&7))*16)-i*1024;
      const int xs=(tb*128+64*j)*1152, fi=FL_H+(lane&1?32+(9*cb>>2)+(lane>>1)%3:2*tb+((lane>>1)&1));
      #pragma unroll 1
      for(int s=0;s<9;s++){ const int o=xs+rot*128;
        if(s==8) asm volatile("buffer_load_dwordx2 v[252:253], %0, %1, %2 offen sc1"::"v"(fi*128),"s"(wsd),"s"(WS_FL):"v252","v253");
        glds4(x,v[0],v[1],v[2],v[3],o,b+j*8192); glds4(x,v[0],v[1],v[2],v[3],o+32*1152,b+j*8192+4096);
        if(s){ wait_vm<8>(); bar(); } rot=rot==8?0:rot+1; b=b==3*34816?0:b+34816; }
      unsigned f0,f1; asm volatile("v_mov_b32 %0, v252\n\tv_mov_b32 %1, v253":"=v"(f0),"=v"(f1));
      const u32x2 tag=mktag(ep);
      if(!__all(f0==tag[0]&&f1==tag[1])) waitflag(ws,fi,tag);
      glds<1>(wsd,j&&lane>=36?0x7ffffff0:lane*16,j?WS_CS+cb*576:WS_RS+tb*1024,LA_ST+j*1024);
      wait_vm<1>(); bar();
    } else { const int j=wave-4; int v[9];
      UNR for(int i=0;i<9;i++){ int c=64*i+lane, r=(c*3641)>>16; v[i]=((r&19)|((r&4)<<1)|((r>>1)&4))*3456+(c-18*r)*16-(i&3)*1024; }
      const int wsb=32*j*3456+cb*288;
      #pragma unroll 1
      for(int s=0;s<9;s++){ const int o=wsb+rot*(64*3456), l=b+16384+j*9216;
        glds4(wqkv,v[0],v[1],v[2],v[3],o,l); glds4(wqkv,v[4],v[5],v[6],v[7],o,l+4096); glds<0>(wqkv,v[8],o,l+8192);
        if(s){ wait_vm<9>(); bar(); } rot=rot==8?0:rot+1; b=b==3*34816?0:b+34816; }
      wait_vm<0>(); bar();
    }
    wait_vm<0>(); bar();
  } else {
    f32x32 a0={},a1={}; f32x8 a2={}; int ea[9][2];
    UNR for(int j=0;j<9;j++) UNR for(int e=0;e<2;e++){ int col=j*16+4*g+2*e; ea[j][e]=LA_O+((col*57)>>10)*4572+2*col+(32*wave+tok)*36; asm volatile("":"+v"(ea[j][e])); }
    const int xb=(32*wave+tok)*128+((g^((tok>>1)&7))*16), wb=16384+(16*(g>>1)+4*(g&1)+(tok>>2))*288+(lane&3)*8;
    int x0=xb,x1=xb^64,w0=wb,w1=wb+9216,sb=0,it=-9;
    if(!(OPT&1)) UNR for(int i=0;i<3;i++){ const int hh=3*(cb>>2)+i, h0=8*(cb&3), pr=4*tb+wave;
      st16(ws,hh<8?lane*16:(lane>>3)*8192+(lane&7)*16,hh<8?WS_OM+(h0+hh)*65536+pr*1024:WS_OT+h0*8192+pr*128,(u32x4){~0u,~0u,~0u,~0u}); }
    asm volatile("":"+{v[128:159]}"(a0),"+{v[160:191]}"(a1),"+{v[192:199]}"(a2));
    SB0; bar();
    asm volatile(GA_ASM:"+{v[128:159]}"(a0),"+{v[160:191]}"(a1),"+{v[192:199]}"(a2),[x0]"+&v"(x0),[x1]"+&v"(x1),[w0]"+&v"(w0),[w1]"+&v"(w1),[sb]"+&s"(sb),[it]"+&s"(it):[xb]"v"(xb),[wb]"v"(wb):"memory","scc",GA_CLOB);

    f32x2 st[2]; f32x4 c[9];
    UNR for(int t=0;t<2;t++){ st[t]=LD(f32x2,LA_ST+(32*wave+16*t+tok)*8); if(cb<4) st[t][1]*=0.34004648f; }
    UNR for(int j=0;j<9;j++) c[j]=LD(f32x4,LA_CS+j*64+g*16);
    UNR for(int j=0;j<9;j++) UNR for(int t=0;t<2;t++){ f32x4 v=((j<4?sub4(a0,2*j+t):j<8?sub4(a1,2*j-8+t):sub4(a2,t))+c[j]*st[t][0])*st[t][1];
      UNR for(int e=0;e<2;e++) LD(unsigned,ea[j][e]+t*576)=pk(v[2*e],v[2*e+1]); }
    wait_lgkm0();
  }
  bar();
  { const int tid=wave*64+lane, dst=WS_QKV+cb*8*73728+tb*4608; u32x4 v[5];
    UNR for(int i=0;i<5;i++) if(i<4||wave<4) v[i]=LD(u32x4,LA_O+(tid+512*i)*16);
    UNR for(int i=0;i<5;i++) if(i<4||wave<4){ int c=tid+512*i, hd=(c*3641)>>20; st16(ws,hd*(73728-4608)+c*16,dst,v[i]); } }
}
DI void helperA(RS x,RS wqkv,RS ws,int h,int wave,int lane){
  const int g=lane>>4,tok=lane&15;
  if(h<32){
    u32x4 v[2][5];
    UNR for(int t=0;t<2;t++) UNR for(int i=0;i<5;i++) v[t][i]=__builtin_amdgcn_raw_buffer_load_b128(x,i<4||tok<8?(4*t+g)*1152+tok*16+i*256:0x7ffffff0,(h*64+wave*8)*1152,0);
    UNR for(int t=0;t<2;t++){ f32x4 s={},q={};
      UNR for(int i=0;i<5;i++) UNR for(int e=0;e<2;e++){ f32x4 z=up4(v[t][i][2*e],v[t][i][2*e+1]); s+=z; q+=z*z; }
      float nm=rowsum16(s[0]+s[1]+s[2]+s[3])*(-1.f/576), d=rowsum16(q[0]+q[1]+q[2]+q[3]);
      float rs=__builtin_amdgcn_rsqf(fmaxf(d*(1.f/576)-nm*nm,0.f)+1e-5f);
      st8(ws,tok==0?(4*t+g)*8:0x7ffffff0,WS_RS+(h*64+wave*8)*8,(u32x2){__float_as_uint(nm),__float_as_uint(rs)}); }
  } else if(h<59){
    u32x4 v[9]; f32x4 s[2]={};
    UNR for(int i=0;i<9;i++) v[i]=__builtin_amdgcn_raw_buffer_load_b128(wqkv,(lane>>3)*3456+(lane&7)*16,(wave*72+8*i)*3456+(h-32)*128,0);
    UNR for(int i=0;i<9;i++) UNR for(int e=0;e<2;e++) s[e]+=up4(v[i][2*e],v[i][2*e+1]);
    UNR for(int e=0;e<2;e++) LD(f32x4,(wave*64+lane)*32+e*16)=s[e];
    wait_lgkm0(); bar(); float z=0;
    UNR for(int i=0;i<8;i++) z+=LD(float,(i*8+(lane>>3))*256+wave*32+(lane&7)*4);
    z=x32(x16(dppadd<0x128>(z),false),false);
    __builtin_amdgcn_raw_buffer_store_b32(__float_as_uint(z),ws,lane<8?lane*4:0x7ffffff0,WS_CS+(h-32)*256+wave*32,16);
  }
}

constexpr int LB_K=110592, LB_V=LB_K+24576;
DI int widB(int head,int tb){ return (head>>2)+8*(head&3)+32*tb; }
DI void phaseB(u32x4 wsd,u32x4 wo,RS ws,int wid,int wave,int lane,const int (&vo)[3],u32x4 w1,u32x4 w2){
  const int head=(wid&7)*4+((wid>>3)&3), tb=wid>>5, g=lane>>4, qq=lane&15;
  const int qb=WS_QKV+head*73728;
  u32x4 qf[2];
  UNR for(int u=0;u<2;u++) qf[u]=ld16(ws,(16*u+qq)*36+(g>=2?20:g*16),qb+(tb*256+32*wave)*36);
  { const int t0=tb*256-256+64*wave, so=qb+(t0<0?0:t0)*36;
    UNR for(int i=0;i<3;i++){ int id=i*64+lane, r=(id*171)>>9, c=id-3*r; const int s=r*36+(c==2?20:c*16);
      glds<1>(wsd,s,so+32*73728,LB_K+(wave*3+i)*1024); glds<1>(wsd,s,so+64*73728,LB_V+(wave*3+i)*1024); }
    wait_vm<0>();
    LD(unsigned,LB_K+(wave*64+lane)*48+32)=t0<0?0xfe00u:0u;
    LD(u32x2,LB_V+(wave*64+lane)*48+32)=(u32x2){0x3f803f80u,0x3f803f80u}; }
  wait_lgkm0(); bar();
  if(wave<4) __builtin_amdgcn_s_setprio(1);
  else if(!(OPT&2)) UNR for(int i=0;i<7;i++) if(i<6||wave<7) st16(ws,lane*16,WS_PART+wid*27648+(wave-4+4*i)*1024,(u32x4){~0u,~0u,~0u,~0u});
  if(NBP==0) if(7-wave<NB) pfw(w1,w2,wid,64*NA+(7-wave)*32+(wid>>3),lane);
  const int db=wid>>5;
  UNR for(int u=0;u<2;u++){ if(g>=2){ qf[u][0]=g==2?0x3f80:0; qf[u][1]=0; qf[u][2]=0; } if(g==3) qf[u][3]=0; }
  const unsigned mk0=(4*g>qq?0xffffu:0u)|(4*g+1>qq?0xffff0000u:0u), mk1=(4*g+2>qq?0xffffu:0u)|(4*g+3>qq?0xffff0000u:0u);
  const int kp=LB_K+(32*wave+qq)*48+(g>=2?32:g*16), p=lane&3, vr=LB_V+(32*wave+4*g+(qq>>2))*48, vp=vr+p*8, vt=vr+(p==0?40:32);
  f32x4 S[2][17]; float m[2];
  s16x8 K[18];
  { UNR for(int T=0;T<18;T++) K[T]=LD(s16x8,kp+T*768);
    SB0;
    UNR for(int T=0;T<17;T++) S[0][(T+16)%17]=mfma(K[(T+16)%17],as8(qf[0]),(f32x4){0,0,0,0});
    SB0;
    UNR for(int T=0;T<17;T++) S[1][(T+16)%17]=mfma(K[(T+16)%17+1],as8(qf[1]),(f32x4){0,0,0,0}); }
  auto rmax=[&](int u){ UNR for(int r=0;r<4;r++) S[u][0][r]=(4*g+r>qq)?S[u][0][r]:S[u][16][r];
    float a=max3(S[u][0][0],S[u][0][1],S[u][1][0]), b=max3(S[u][0][2],S[u][0][3],S[u][1][1]);
    a=max3(a,S[u][1][2],S[u][1][3]);
    UNR for(int T=2;T<16;T++){ b=max3(b,S[u][T][0],S[u][T][1]); a=max3(a,S[u][T][2],S[u][T][3]); }
    m[u]=x32(x16(max3(a,b,b),true),true); };
  rmax(0); UNR for(int i=0;i<17;i++){ SGB(0x8,1); SGB(0x2,2); } SB0;
  f32x4 Z[2][17];
  auto sx=[&](int u,int T){ Z[u][T]=mfma(K[T+u],as8(qf[u]),(f32x4){-m[u],-m[u],-m[u],-m[u]}); };
  UNR for(int T=0;T<17;T++) sx(0,(T+16)%17);
  rmax(1); UNR for(int i=0;i<17;i++){ SGB(0x8,1); SGB(0x2,2); } SB0;
  s16x8 Vm[9],Vt[9]; u32x2 P[2][16];
  auto ex=[&](int u,int T){ if(T==0) UNR for(int r=0;r<4;r++) Z[u][0][r]=(4*g+r>qq)?Z[u][0][r]:Z[u][16][r];
    P[u][T][0]=pk(__builtin_amdgcn_exp2f(Z[u][T][0]),__builtin_amdgcn_exp2f(Z[u][T][1])); P[u][T][1]=pk(__builtin_amdgcn_exp2f(Z[u][T][2]),__builtin_amdgcn_exp2f(Z[u][T][3])); };
  UNR for(int k=0;k<9;k++){ Vm[k]=cat(trread(vp+2*k*768),trread(vp+(2*k+1)*768)); Vt[k]=cat(trread(vt+2*k*768),trread(vt+(2*k+1)*768));
    if(k<8){ ex(0,2*k); ex(0,2*k+1); } if(k==7){ sx(1,16); sx(1,0); } if(k<7) SB0; }
  SB0;
  if(NBP==3) if(7-wave<NB) pfw(w1,w2,wid,64*NA+(7-wave)*32+(wid>>3),lane);
  f32x4 o[2]={},t4[2]={};
  auto pv=[&](int u,int k){ u32x4 pb;
    if(u==0) pb= k==0?(u32x4){P[0][0][0]&mk0,P[0][0][1]&mk1,P[0][1][0],P[0][1][1]} : k==8?(u32x4){P[0][0][0]&~mk0,P[0][0][1]&~mk1,0,0} : (u32x4){P[0][2*k&15][0],P[0][2*k&15][1],P[0][(2*k+1)&15][0],P[0][(2*k+1)&15][1]};
    else     pb= k==0?(u32x4){0,0,P[1][0][0]&mk0,P[1][0][1]&mk1} : k==8?(u32x4){P[1][15][0],P[1][15][1],P[1][0][0]&~mk0,P[1][0][1]&~mk1} : (u32x4){P[1][(2*k-1)&15][0],P[1][(2*k-1)&15][1],P[1][2*k&15][0],P[1][2*k&15][1]};
    o[u]=mfma(Vm[k],as8(pb),o[u]); t4[u]=mfma(Vt[k],as8(pb),t4[u]); };
  UNR for(int k=0;k<9;k++){ if(k<8) sx(1,2*k+1); if(k<7) sx(1,2*k+2); pv(0,k); if(k>0) pv(1,k-1); if(k>0) ex(1,2*k-1); if(k<8) ex(1,2*k);
    UNR for(int i=0;i<6;i++){ SGB(0x8,1); SGB(0x402,2); } SB0; }
  pv(1,8);
  u32x4 om; u32x2 ot;
  UNR for(int u=0;u<2;u++){ float inv=__builtin_amdgcn_rcpf(__uint_as_float(__builtin_amdgcn_permlane16_swap(__float_as_uint(t4[u][0]),__float_as_uint(t4[u][0]),false,false)[1])); f32x4 z=o[u]*inv; om[2*u]=pk(z[0],z[1]); om[2*u+1]=pk(z[2],z[3]); ot[u]=pk(t4[u][2]*inv,t4[u][3]*inv); }
  om[0]=min(om[0],~1u); om[3]=min(om[3],~1u); ot[0]=min(ot[0],~1u); ot[1]=min(ot[1],~1u);
  if(!(OPT&2)&&wave>=4) wait_vm<0>();
  st16(ws,lane*16,WS_OM+head*65536+(tb*8+wave)*1024,om);
  st8(ws,g==0?lane*8:0x7ffffff0,WS_OT+head*8192+(tb*8+wave)*128,ot);
  __builtin_amdgcn_s_setprio(0);
  if(NBP==2) if(7-wave<NB) pfw(w1,w2,wid,64*NA+(7-wave)*32+(wid>>3),lane);
  if(wid<192&&wave<4){
    #pragma unroll 1
    for(int h=wave+4*WOE;h<32;h+=4) UNR for(int k=0;k<3;k++) glds<0>(wo,vo[k],h*(18*1152)+db*192,h*3072+k*1024);
    UNR for(int k=0;k<3;k++){ int r=((k*64+lane)*5462)>>16; glds<0>(wo,vo[k]+(((r&12)+((r>>1)&1))*18+(r&1)-r)*1152,((8*(wave&2)+2*(wave&1))*18+16)*1152+db*192,(32+wave)*3072+k*1024); }
  }
}

constexpr int LE_H=G_LF+6*9216, LE_ACC=16384;
DI int tline(int t,int h){ return (t>>2)*8+(t&3)+4*h; }
DI int fo16(u32x4 wsd,int lane,int j){ return lane*16+((EXP&8)==0&&(((wsd[0]>>7)+(lane>>3))&7)==3?WS_OV+128*(1+j%7):0); }
struct LS{ int lb,so; };
#define L_OUT [t]"=&s"(t),[lb]"+&s"(s.lb),[so]"+&s"(s.so)
template<int PH,int LDW,int NP=12,int SL=0> DI void g_wload(u32x4 W,LS& s,int j,int lane,u32x4 hd=(u32x4){},int hs=0){
  int wp[4],t; UNR for(int i=0;i<4;i++){ int row=4*i+(lane>>4); const int c=(lane&15)^((row&7)<<1); wp[i]=row*LDW+(c&7)*16+(c>>3)*512-i*1024; }
#define W_IN [d]"s"(W),[v0]"v"(wp[0]),[v1]"v"(wp[1]),[v2]"v"(wp[2]),[v3]"v"(wp[3]),[l0]"s"(j*4096),[ls]"n"(8192),[le]"n"(12*8192),[ks]"n"((EXP&32)?0:32*LDW)
  if(PH==0&&NP>0) asm volatile(LWDP_ASM:L_OUT:W_IN,[np]"n"(-NP),[sl]"n"(SL):"memory","scc");
  if(PH==1) asm volatile(LWD_ASM:L_OUT:W_IN:"memory","scc");
  if(PH==2) asm volatile(LWE_ASM:L_OUT:W_IN,[hd]"s"(hd),[hs]"s"(hs+j*4096),[hl]"s"(LE_H+j*4096),[hv]"v"(lane*16):"memory","scc");
}
template<int DT,int FC,int RF> DI void g_fload(u32x4 wsd,int so,int j,int v){
  const int first=j?DT-FC:0, l0=G_LF+first*1024; LS s={l0,so+first*1024}; int t;
#define F_IN [d]"s"(wsd),[v0]"v"(v),[v1]"v"(v+4096),[l0]"s"(l0),[ls]"n"(DT*1024),[le]"n"(G_LF+RF*DT*1024),[ks]"n"((EXP&16)?0:36864)
  if(FC==3) asm volatile(LF3P_ASM LF3_ASM:L_OUT:F_IN:"memory","scc");
  if(FC==4) asm volatile(LF4P_ASM LF4_ASM:L_OUT:F_IN:"memory","scc");
  if(FC==5) asm volatile(LF5P_ASM LF5_ASM:L_OUT:F_IN:"memory","scc");
}
#define G_SETUP(NIT) \
  const int row_=4*g+((lane&15)>>2), wb0=row_*256+(((4*wave+((lane&3)>>1))^((row_&7)<<1))*16)+(lane&1)*8, wb1=wb0^32, fb=G_LF+lane*16; \
  int wa0=wb0,wa1=wb1,fa=fb,sw=0,sf=0,it=-(NIT); SB0; bar();
#define G_OUT [wa0]"+&v"(wa0),[wa1]"+&v"(wa1),[fa]"+&v"(fa),[sw]"+&s"(sw),[sf]"+&s"(sf),[it]"+&s"(it)
#define G_IN [wb0]"v"(wb0),[wb1]"v"(wb1),[fb]"v"(fb)

constexpr int LC_R=110592;
DI void phaseC(RS x,u32x4 wsd,RS ws,int wid,int wave,int lane,u32x2 tag2){
  const int db=wid>>5, tb=(wid&7)*4+((wid>>3)&3), g=lane>>4, tok=lane&15; const unsigned tag=tag2[0];
  const int pr=wave&1, dh=wave>>1, q=tok>>2, p=lane&3;
  u32x4 xa[3],id[2]; int wof[6];
  UNR for(int j=0;j<3;j++) xa[j]=__builtin_amdgcn_raw_buffer_load_b128(x,(16*(g>>1)+tok)*1152+(g&1)*16+j*32,(tb*64+32*pr)*1152+db*192+dh*96,0);
  UNR for(int t=0;t<2;t++) UNR for(int e=0;e<4;e++) id[t][e]=(g==2*t+(tok>>3)&&e==((tok&7)>>1))?0x3f80u<<(16*(tok&1)):0u;
  UNR for(int j=0;j<6;j++) wof[j]=dh*49152+(4*g+q)*192+(((((j<3?3*dh:-3*dh)+j)*2+(p>>1))^((g&1)<<1))*16)+(p&1)*8;
  f32x4 acc[2][3],oth[2][3]; s16x8 w[2][6];
  auto rw=[&](int slot,int o){ UNR for(int j=0;j<6;j++) w[slot][j]=cat(trread(o+wof[j]),trread(o+3072+wof[j])); };
  const int mp=WS_OM+dh*1048576+(2*tb+pr)*1024, tp=WS_OT+dh*131072+(2*tb+pr)*128;
  if(!(OPT&1)) waitflag(ws,FL_A+widA(tb>>1,tok%12),tag2);
  wait_vm<0>(); bar();
  _Pragma("unroll 1") for(int it=0;it<SPIN_MAX;it++){ asm volatile("":::"memory");
    u32x4 om[16]; u32x2 ot[4]; unsigned mx=0;
    UNR for(int k=0;k<4;k++) ot[k]=__builtin_amdgcn_raw_buffer_load_b64(ws,g*32768+tok*8,tp+k*8192,16);
    UNR for(int h=0;h<CUP;h++){ SB0; om[h]=ld16(ws,lane*16,mp+h*65536); }
    SB0; rw(0,98304-dh*43008); u32x4 a[2][2];
    auto ra=[&](int s){ UNR for(int t=0;t<2;t++){ u32x4 z=s?(u32x4){om[(2*s-2)&15][2*t],om[(2*s-2)&15][2*t+1],om[(2*s-1)&15][2*t],om[(2*s-1)&15][2*t+1]}:(u32x4){ot[0][t],ot[1][t],ot[2][t],ot[3][t]};
      mx=max(max(mx,z[t]),z[2+t]); if(s==0) mx=max(max(mx,z[1-t]),z[3-t]); a[s&1][t]=z; } };
    UNR for(int j=0;j<3;j++) UNR for(int t=0;t<2;t++){ acc[t][j]=mfma(as8(xa[j]),as8(id[t]),(f32x4){}); oth[t][j]=(f32x4){}; }
    SB0; ra(0); SB0;
    if(!__all(mx!=~0u)){
      _Pragma("unroll 1") for(;it<SPIN_MAX-2;it++){ asm volatile("":::"memory");
        if(__all(__builtin_amdgcn_raw_buffer_load_b32(ws,tok*8192,tp+124,16)!=~0u)) break; __builtin_amdgcn_s_sleep(CSL); }
      continue; }

    UNR for(int s=0;s<9;s++){
      if(s<8) rw((s+1)&1,s*6144);
      UNR for(int j=0;j<3;j++) UNR for(int t=0;t<2;t++) oth[t][j]=mfma(as8(a[s&1][t]),w[s&1][3+j],oth[t][j]);
      if(s<8) UNR for(int i=0;i<6;i++){ SGB(0x8,1); SGB(0x100,2); }
      SB0; UNR for(int h=CUP+2*s;h<CUP+2*s+2;h++) if(h<16){ om[h]=ld16(ws,lane*16,mp+h*65536); SB0; }
      if(s<8) ra(s+1);
      UNR for(int j=0;j<3;j++) UNR for(int t=0;t<2;t++) acc[t][j]=mfma(as8(a[s&1][t]),w[s&1][j],acc[t][j]);
      if(s<8){ SGB(0x8,1); UNR for(int i=0;i<5;i++){ SGB(0x8,1); SGB(0x2,2); } }
      SB0; }
    if(__all(mx!=~0u)) break; }

  UNR for(int j=0;j<3;j++) UNR for(int t=0;t<2;t++) LD(f32x4,LC_R+wave*6144+(j*2+t)*1024+lane*16)=oth[t][j];
  wait_lgkm0(); bar();
  UNR for(int j=0;j<3;j++) UNR for(int t=0;t<2;t++) acc[t][j]+=LD(f32x4,LC_R+(wave^2)*6144+(j*2+t)*1024+lane*16);
  float sm_,sq_;
  { f32x4 y[2]; f32x2 z[2]; const bool b3=tok&8,b2=tok&4;
    UNR for(int e=0;e<2;e++){ f32x4 v[2]; UNR for(int t=0;t<2;t++) v[t]=e?acc[t][0]*acc[t][0]+acc[t][1]*acc[t][1]+acc[t][2]*acc[t][2]:acc[t][0]+acc[t][1]+acc[t][2];
      UNR for(int r=0;r<4;r++) y[e][r]=(b3?v[1][r]:v[0][r])+dpp<0x128>(b3?v[0][r]:v[1][r]);
      UNR for(int r=0;r<2;r++) z[e][r]=(b2?y[e][2+r]:y[e][r])+dpp<0x141>(b2?y[e][r]:y[e][2+r]); }
    sm_=dppadd<0x4e>(((tok&1)?z[0][1]:z[0][0])+dpp<0xb1>((tok&1)?z[0][0]:z[0][1]));
    sq_=dppadd<0x4e>(((tok&1)?z[1][1]:z[1][0])+dpp<0xb1>((tok&1)?z[1][0]:z[1][1])); }
  const int sb=WS_ST+tb*12288+pr*512;
  st16(ws,tok&2?0x7ffffff0:(16*(tok>>3)+4*g+2*((tok>>2)&1)+(tok&1))*16,sb+(db*2+dh)*1024,(u32x4){__float_as_uint(sm_),tag,__float_as_uint(sq_),~tag});
  const int fo=((2*tb+pr)*36+6*db+3*dh)*1024; SB0;
  UNR for(int j=0;j<3;j++) st16(ws,lane*16+j*1024,WS_HF+fo,(u32x4){pk(acc[0][j][0],acc[0][j][1]),pk(acc[0][j][2],acc[0][j][3]),pk(acc[1][j][0],acc[1][j][1]),pk(acc[1][j][2],acc[1][j][3])});
  u32x4 gr[6]; const int gp=(tok>>3)*6144+(16*((tok>>2)&1)+4*g+(tok&3))*16;
  if(SDL) __builtin_amdgcn_s_sleep(SDL);
  _Pragma("unroll 1") for(int it=0;it<SPIN_MAX;it++){ asm volatile("":::"memory"); UNR for(int k=0;k<6;k++) gr[k]=ld16(ws,gp+(k&3)*1024,sb+(k>>2)*4096);
    bool ok=true; UNR for(int k=0;k<6;k++) ok&=(gr[k][1]==tag)&(gr[k][3]==~tag);
    if(__all(ok)) break; }
  float mu=0,m2=0; UNR for(int k=0;k<6;k++){ mu+=__uint_as_float(gr[k][0]); m2+=__uint_as_float(gr[k][2]); }
  mu=dppadd<0x128>(mu)*(1.f/576); m2=fmaxf(dppadd<0x128>(m2)*(1.f/576)-mu*mu,0.f);
  float r1=__builtin_amdgcn_rsqf(m2+1e-5f), rr=r1*__builtin_amdgcn_rsqf(m2*r1*r1+1e-5f);
  u32x4 hn[3];
  UNR for(int t=0;t<2;t++){ f32x4 m4=t?bc4<4>(mu):bc4<0>(mu), r4=t?bc4<4>(rr):bc4<0>(rr);
    UNR for(int j=0;j<3;j++){ f32x4 z=(acc[t][j]-m4)*r4; hn[j][2*t]=pk(z[0],z[1]); hn[j][2*t+1]=pk(z[2],z[3]); } }
  UNR for(int j=0;j<3;j++) st16(ws,fo16(wsd,lane,2*db+dh)+j*1024,WS_HN+fo,hn[j]);
  wait_vm<0>(); bar(); if(wave==0) setflag(ws,FL_C+wid,tag2);
}

__global__ __launch_bounds__(512,2) void fused(const char* x,const char* wqkv,const char* wo,const char* w1,const char* b1,const char* w2,const char* b2,char* out,char* ws_){
#if __HIP_DEVICE_COMPILE__
  SB0;
  u64 pc; unsigned len; asm volatile("s_getpc_b64 %0\n.Lfused_pc:\n\ts_mov_b32 %1, .Lfused_end-.Lfused_pc":"=s"(pc),"=s"(len));
  const int wave=__builtin_amdgcn_readfirstlane(threadIdx.x>>6), lane=threadIdx.x&63, wid=blockIdx.x, g=lane>>4;
  const RS ws=mkrs(ws_,WS_TS+65536); const u32x4 wsd=mkd(ws_,WS_TS), xd=mkd(x,2359296);

  const u64 ep=*(const __attribute__((address_space(4))) u64*)(ws_+WS_FL+FL_EPOCH*128);
  const u32x4 w2d=mkd(w2,18874368), w1d=mkd(w1,18874368); const int hw=((wid>>3)-24)*8+wave;
  if(__builtin_expect(wid<192,1)) phaseA(xd,mkd(wqkv,1990656),wsd,ws,wid,wave,lane,ep);
  else {
    if(PFC&&wave==7){
      pf1(mkd((const void*)pc,len),((hw>>3)*64+lane)*128); }
    if(PFS&&wid>=251) UNR for(int i=0;i<2;i++){ const int id=(wid-251)*8+wave+40*i;
      if(id<64) asm volatile("buffer_load_dword v255, %0, %1, %2 offen sc1"::"v"(lane*128),"s"(wsd),"s"(id<16?WS_FL+id*8192:WS_ST+(id-16)*8192):"v255"); }
    helperA(mkrs(x,2359296),mkrs(wqkv,1990656),ws,wid-192,wave,lane);
    if(OPT&1) UNR for(int i=0;i<5;i++)
      st16(ws,i<4?lane*16:lane<32?(lane>>3)*8192+(lane&7)*16:0x7ffffff0,i<4?WS_OM+(4*wave+i)*65536+(wid-192)*1024:WS_OT+4*wave*8192+(wid-192)*128,(u32x4){~0u,~0u,~0u,~0u}); }
  const u32x2 tag=mktag(ep);
  wait_vm<0>(); bar(); if(wave==0) setflag(ws,FL_A+wid,tag);
  if(wid<192){ } else {
    if(hw<11) pf1(mkd(wo,663552),(((wid&7)*11+hw)*64+lane)*128);
    if(PFT&&hw>=16&&hw<16+PFT) pf1(wsd,((hw-16)*64+lane)*(WS_TS/64/PFT));
    if(PFT&&hw==12) pf1(mkd(out,2359296),lane*65536);
    UNR for(int i=0;i<NA;i++) pfw(w1d,w2d,wid,hw+64*i,lane);
    if(OPT&8) UNR for(int i=0;i<2;i++) if(i==0||hw<17) pf1(mkd(wo,663552),((hw+64*i)*64+lane)*128);
    wait_vm<0>(); }
  int vo[3]; UNR for(int k=0;k<3;k++){ int id=k*64+lane, r=(id*5462)>>16, c=(id-12*r)^(((r>>2)&1)<<1); vo[k]=r*1152+c*16; }
  {
    const int head=(wid&7)*4+((wid>>3)&3), tb=wid>>5, t=2*tb-2+(lane&3), ten=(lane>>2)%3;
    if(FD1&&wave==6) __builtin_amdgcn_s_sleep(FD1);
    if(wave==6) waitflag(ws,(OPT&1)&&lane>=32?FL_H+(lane>=48?2*head+(lane&1):8*tb+(lane&7)):FL_A+widA(t<0?0:t,(head+32*ten)>>3),tag);
    bar(); }
  phaseB(wsd,mkd(wo,663552),ws,wid,wave,lane,vo,w1d,w2d);
  if(wave>=4||wid>=192){ if(CV&1) wait_vm<0>(); bar(); }
  const int did=(wid&7)*27+((wid>>3)+27-((DROT>>(wid&7)*4)&15))%27, ddb=did%6, dnb=did/6;
  const int kq=(wid&7)>>1, sb=(wid&1)*8+(wid>>5), db=(wid>>3)&3, es=2*tline(sb,kq>>1)+(kq&1);
  auto fd=[&](int l){ int n=18*kq+(l>>1); return FL_D+((n>>3)*4+(n&3))*6+(3*db>>1)+(l&1); };
  float bb[2]; UNR for(int n=0;n<2;n++){ bb[n]=blo(*(const u16*)(b2+(es*32+n*16+(lane&15))*2)); }
  if(wid>=192&&!(EXP&64)){ if(PDL) _Pragma("unroll 1") for(int i=0;i<PDL;i++) __builtin_amdgcn_s_sleep(16);
    UNR for(int i=0;i<NC;i++) pfw(w1d,w2d,wid,288+hw+64*i,lane);
    if(wid>=216) for(int n=288+64*NC+hw-24;n<608;n+=40) pfw(w1d,w2d,wid,n,lane);
    if((OPT&2)&&wid>=224){ const int c=(wid-224)*8+wave;
      waitflag(ws,FL_C+(c>>1),tag);
      _Pragma("unroll 1") for(int i=0;i<27;i++){ st16(ws,lane*16,WS_PART+c*27648+i*1024,(u32x4){~0u,~0u,~0u,~0u}); __builtin_amdgcn_s_sleep(PSL); }
      wait_vm<0>(); setflag(ws,FL_E+c,tag); }
    if(wid>=216) for(int n=64*NA+32*NB+hw-24;n<288;n+=40) pfw(w1d,w2d,wid,n,lane);
    wait_vm<0>(); }
  if(wave>=4){
    if(wid<192) bar();
    if(wave<6){ const int j=wave-4;
      if(wid<216){ LS s={j*4096,tline(dnb,0)*128+16*j*9216}; const u32x4 W=w1d;
        g_wload<0,9216,DN1,DSL>(W,s,j,lane); if(wid<192) bar(); g_wload<0,9216,12-DN1>(W,s,j,lane);
        wait_vm<40>(); bar(); if(j==0) waitflag(ws,FL_C+ddb*32+(lane&31),tag);
        g_wload<1,9216>(W,s,j,lane); }
      LS s={j*4096,(kq*1152+16*j)*4096+tline(sb,0)*128}; const u32x4 W=w2d;
      g_wload<0,4096,EN1,ESL2>(W,s,j,lane); if(wid<216) bar(); g_wload<0,4096,12-EN1>(W,s,j,lane);
      wait_vm<40>(); bar(); if(j==0) waitflag(ws,(OPT&2)&&lane>=48?FL_E+(wid&~6)+(lane&6):fd(lane%36),tag);
      g_wload<2,4096>(W,s,j,lane,wsd,WS_HF+(es*36+db*9)*1024);
    } else {
      if(wid<192) bar();
      if(wid<216){ if(FD2) __builtin_amdgcn_s_sleep(FD2); waitflag(ws,FL_C+ddb*32+((EXP&4)?(lane&31):(lane&3)*8),tag); g_fload<6,3,8>(wsd,WS_HN+ddb*6144,wave-6,fo16(wsd,lane,2*ddb+wave-6)); bar(); }
      waitflag(ws,fd((EXP&4)?lane%36:lane%6),tag);
      const int F=WS_U+(kq*36*36+db*9)*1024;
      const int v=fo16(wsd,lane,2*db+wave-6);
      if(wave==6) g_fload<9,5,6>(wsd,F,0,v); else g_fload<9,4,6>(wsd,F,1,v);
    }
  } else {
    if(wid<192) phaseC(mkrs(x,2359296),wsd,ws,wid,wave,lane,tag);
    if(wid<216){
      const int ds=2*tline(dnb,wave>>1)+(wave&1);
      f32x16 a0; { f32x4 b[2]; UNR for(int n=0;n<2;n++){ u32x2 v=*(const u32x2*)(b1+(ds*32+n*16+g*4)*2); b[n]=up4(v[0],v[1]); } UNR for(int i=0;i<16;i++) a0[i]=b[(i>>2)&1][i&3]; }
      f32x16 a1=a0,a2=a0; int ua[6];
      UNR for(int d=0;d<6;d++){ const int dt=ddb*6+d, q=dt/9; ua[d]=fo16(wsd,lane,2*q+(dt-9*q>=5))+d*1024; asm volatile("":"+v"(ua[d])); }
      asm volatile("":"+{v[128:143]}"(a0),"+{v[144:159]}"(a1),"+{v[160:175]}"(a2));
      G_SETUP(16)
      asm volatile(GD_ASM:"+{v[128:143]}"(a0),"+{v[144:159]}"(a1),"+{v[160:175]}"(a2),G_OUT:G_IN:"memory","scc",GD_CLOB);
      const int dst=WS_U+(ds*36+ddb*6)*1024;
      UNR for(int d=0;d<6;d++){ u32x4 o;
        UNR for(int n=0;n<2;n++){ f32x4 v=sub4(d<2?a0:d<4?a1:a2,(d&1)*2+n), z=v*(v*v*-0.10294324f-2.3022082f), e;
          UNR for(int r=0;r<4;r++) e[r]=__builtin_amdgcn_exp2f(z[r]);
          e+=1.f; UNR for(int r=0;r<4;r++) e[r]=__builtin_amdgcn_rcpf(e[r]);
          v*=e; o[2*n]=pk(v[0],v[1]); o[2*n+1]=pk(v[2],v[3]); }
        st16(ws,ua[d],dst,o); SB0; }
      wait_vm<0>(); bar(); if(wave==0) setflag(ws,FL_D+did,tag);
    }
    f32x32 a0={},a1={}; f32x8 a2={};
    asm volatile("":"+{v[128:159]}"(a0),"+{v[160:191]}"(a1),"+{v[192:199]}"(a2));
    G_SETUP(6)
    asm volatile(GE_ASM:"+{v[128:159]}"(a0),"+{v[160:191]}"(a1),"+{v[192:199]}"(a2),G_OUT:G_IN:"memory","scc",GE_CLOB);
    f32x4 acc[9][2]; UNR for(int d=0;d<9;d++) UNR for(int n=0;n<2;n++) acc[d][n]=d<4?sub4(a0,d*2+n):d<8?sub4(a1,(d-4)*2+n):sub4(a2,n);
    if(wave!=kq){ const int dst=WS_PART+(wid+2*(wave-kq))*27648+((kq-wave-1)&3)*9216;
      UNR for(int d=0;d<9;d++)
        st16(ws,lane*16+(d&3)*1024,dst+(d>>2)*4096,(u32x4){min(pk(acc[d][0][0],acc[d][0][1]),~1u),pk(acc[d][0][2],acc[d][0][3]),pk(acc[d][1][0],acc[d][1][1]),min(pk(acc[d][1][2],acc[d][1][3]),~1u)});
    } else { UNR for(int d=0;d<9;d++) UNR for(int n=0;n<2;n++) LD(f32x4,LE_ACC+(d*2+n)*1024+lane*16)=acc[d][n]; wait_lgkm0(); }
  }
  bar();
  if(wid==0&&wave==1) setflag(ws,FL_EPOCH,tag);
  if(wave>=4||wave==kq){
    const int d0=wave>=4?2*wave-8:8, src=WS_PART+wid*27648+d0*1024, v1=wave>=4?lane*16+1024:lane*16; u32x4 pv[3][2]; f32x4 v[2][2];
    UNR for(int e=0;e<2;e++) UNR for(int n=0;n<2;n++){ u32x2 hv=__builtin_bit_cast(u32x2,trread(LE_H+(d0+e)*1024+((lane&3)*16+4*g+((lane&15)>>2))*16+n*8));
      v[e][n]=LD(f32x4,LE_ACC+((d0+e)*2+n)*1024+lane*16)+up4(hv[0],hv[1])+bb[n]; }
    if(FD3) __builtin_amdgcn_s_sleep(FD3);
    int got=0; UNR for(int k=0;k<3;k++) pv[k][0]=pv[k][1]=(u32x4){~0u,~0u,~0u,~0u};
    _Pragma("unroll 1") for(int it=0;it<SPIN_MAX;it++){ asm volatile("":::"memory");
      int m=(int)__builtin_amdgcn_ballot_w64(__builtin_amdgcn_raw_buffer_load_b32(ws,lane<3?lane*9216+LPO:0x7ffffff0,WS_PART+wid*27648,16)!=~0u)&7;
      if(!EPR&&m!=7) m=0;
      UNR for(int k=0;k<3;k++) if((m&~got)>>k&1){ pv[k][0]=ld16(ws,lane*16,src+k*9216); pv[k][1]=ld16(ws,v1,src+k*9216); }
      got|=m; if(got==7) break; __builtin_amdgcn_s_sleep(FD4); }

    _Pragma("unroll 1") for(int it=0;it<SPIN_MAX;it++){ bool ok=true;
      UNR for(int k=0;k<3;k++) UNR for(int e=0;e<2;e++) ok&=(pv[k][e][0]!=~0u)&(pv[k][e][3]!=~0u);
      if(__all(ok)) break; __builtin_amdgcn_s_sleep(ESL); asm volatile("":::"memory");
      UNR for(int k=0;k<3;k++){ pv[k][0]=ld16(ws,lane*16,src+k*9216); pv[k][1]=ld16(ws,v1,src+k*9216); } }

    const RS od=mkrs(out,2359296);
    UNR for(int n=0;n<2;n++){ u32x2 o[2];
      UNR for(int e=0;e<2;e++){ UNR for(int k=0;k<3;k++) v[e][n]+=up4(pv[k][e][2*n],pv[k][e][2*n+1]); o[e]=(u32x2){pk(v[e][n][0],v[e][n][1]),pk(v[e][n][2],v[e][n][3])}; }
      auto r0=__builtin_amdgcn_permlane16_swap(o[0][0],o[1][0],false,false), r1=__builtin_amdgcn_permlane16_swap(o[0][1],o[1][1],false,false);
      u32x4 w={r0[0],r1[0],r0[1],r1[1]};
      __builtin_amdgcn_raw_buffer_store_b128(w,od,wave<4&&(g&1)?0x7ffffff0:(n*16+(lane&15))*1152+(g&1)*32+(g>>1)*16,es*32*1152+(db*144+d0*16)*2,OAUX); asm volatile("s_nop 1"::"v"(w)); }
  }
  asm volatile(".Lfused_end:");

#endif
}

extern "C" void kernel_launch(void* const* d_in,const int* in_sizes,int n_in,void* d_out,int out_size,void* d_ws,size_t ws_size,hipStream_t stream){
  (void)hipFuncSetAttribute((const void*)fused,hipFuncAttributeMaxDynamicSharedMemorySize,163840);
  fused<<<256,512,163840,stream>>>((const char*)d_in[0],(const char*)d_in[1],(const char*)d_in[2],(const char*)d_in[4],(const char*)d_in[5],(const char*)d_in[6],(const char*)d_in[7],(char*)d_out,(char*)d_ws);
}
```
